```python
import torch

def _softmax(x, axis=-1):
    return torch.softmax(x, dim=axis)
def _gelu(x):
    return torch.nn.functional.gelu(x, approximate="tanh")
def _silu(x):
    return torch.nn.functional.silu(x)
def _sigmoid(x):
    return torch.sigmoid(x)
def _layernorm(x, axis=-1, eps=1e-5):
    m = x.mean(dim=axis, keepdim=True)
    v = x.var(dim=axis, keepdim=True, unbiased=False)
    return (x - m) / torch.sqrt(v + eps)
def _scatter_add(tgt, idx, src, axis=0):
    return tgt.clone().index_add_(axis, idx, src)
def _segment_sum(src, seg, n):
    out = torch.zeros((n,) + src.shape[1:], dtype=src.dtype)
    return out.index_add_(0, seg, src)
def _pad(x, pw):
    flat = tuple(v for pair in reversed(pw) for v in pair)
    return torch.nn.functional.pad(x, flat)

kernel_name = 'gated_mix__sliding_block_cached__glu_gated_geglu__decode'

def setup_inputs():

    _w = (lambda a: (lambda t: t.to(torch.bfloat16) if t.is_floating_point() else t)(torch.as_tensor(a)))
    def _A_setup():
        B, Sq, D = 4, 1, 640
        Hq, Hkv, Sk, Dh = 32, 32, 4096, 128
        assert Hq % Hkv == 0, (Hq, Hkv)
        return {
            "x": _w((lambda *s: torch.randn(*s, dtype=torch.bfloat16) * 0.05)(B, Sq, D)),
            "k_cache": _w((lambda *s: torch.randn(*s, dtype=torch.bfloat16) * 6.4)(B, Hkv, Sk, Dh)),
            "v_cache": _w((lambda *s: torch.randn(*s, dtype=torch.bfloat16) * 0.05)(B, Hkv, Sk, Dh)),
            "wq": _w((lambda *s: torch.randn(*s, dtype=torch.bfloat16) * 6.4)(D, Hq * Dh) * (D ** -0.5)),
            "wo": _w((lambda *s: torch.randn(*s, dtype=torch.bfloat16) * 0.05)(Hq * Dh, D) * ((Hq * Dh) ** -0.5)),
        }
    _A_in = _A_setup()

    def _B_setup():
        B, S, Dm, Df = 4, 1, 640, 1728
        return {
            "x": _w((lambda *s: torch.randn(*s, dtype=torch.bfloat16) * 0.05)(B, S, Dm)),
            "w_gate": _w((lambda *s: torch.randn(*s, dtype=torch.bfloat16) * 0.8)(Dm, Df) * (Dm ** -0.5)),
            "w_up": _w((lambda *s: torch.randn(*s, dtype=torch.bfloat16) * 0.8)(Dm, Df) * (Dm ** -0.5)),
            "w_down": _w((lambda *s: torch.randn(*s, dtype=torch.bfloat16) * 0.8)(Df, Dm) * (Df ** -0.5)),
        }
    _B_in = _B_setup()

    return {
        "A_x": _A_in["x"],
        "A_k_cache": _A_in["k_cache"],
        "A_v_cache": _A_in["v_cache"],
        "A_wq": _A_in["wq"],
        "A_wo": _A_in["wo"],
        "B_x": _B_in["x"],
        "B_w_gate": _B_in["w_gate"],
        "B_w_up": _B_in["w_up"],
        "B_w_down": _B_in["w_down"],
        "gate_w": _w((lambda *s: torch.randn(*s, dtype=torch.bfloat16) * 0.05)(640) * 16.0),
    }

def reference(A_x, A_k_cache, A_v_cache, A_wq, A_wo, B_x, B_w_gate, B_w_up, B_w_down, gate_w):
    def _A_ref(x, k_cache, v_cache, wq, wo):
        B, Sq, D = x.shape
        Hq, Dh = 32, 128
        Hkv, Sk = k_cache.shape[1], k_cache.shape[2]
        W = min(4096, max(256, Sk // 4))
        rep = Hq // Hkv
        q = (lambda x, *d: x.permute(*d))((x @ wq).reshape(B, Sq, Hq, Dh), 0, 2, 1, 3)
        k = (lambda x, n, axis: torch.repeat_interleave(x, n, dim=axis))(k_cache[:, :, Sk - W:, :], rep, axis=1)
        v = (lambda x, n, axis: torch.repeat_interleave(x, n, dim=axis))(v_cache[:, :, Sk - W:, :], rep, axis=1)
        scores = torch.einsum("bhqd,bhkd->bhqk", q, k) * (Dh ** -0.5)
        p = _softmax(scores, axis=-1)
        o = torch.einsum("bhqk,bhkd->bhqd", p, v)
        return (lambda x, *d: x.permute(*d))(o, 0, 2, 1, 3).reshape(B, Sq, Hq * Dh) @ wo

    def _B_ref(x, w_gate, w_up, w_down):
        g = torch.einsum("bsd,df->bsf", x, w_gate)
        u = torch.einsum("bsd,df->bsf", x, w_up)
        h = _gelu(g) * u
        return torch.einsum("bsf,fd->bsd", h, w_down)

    x = A_x
    g = _sigmoid((x * gate_w).sum(-1, keepdims=True))
    a = _A_ref(x, A_k_cache, A_v_cache, A_wq, A_wo)
    b = _B_ref(x, B_w_gate, B_w_up, B_w_down)
    return g * a + (1 - g) * b
```

```cpp
#include <hip/hip_runtime.h>
#include <hip/hip_bf16.h>
#include <stdint.h>

typedef __attribute__((ext_vector_type(4))) unsigned u32x4;
typedef __attribute__((ext_vector_type(2))) unsigned u32x2;
typedef __attribute__((ext_vector_type(4))) float f32x4;
typedef __attribute__((ext_vector_type(2))) float f32x2;
typedef __attribute__((ext_vector_type(8))) __bf16 bf16x8;
typedef __attribute__((ext_vector_type(2))) __bf16 bf16x2;
typedef unsigned short u16;

constexpr int DM = 640, NH = 32, SK = 4096, WIN = 1024, DH = 128, DF = 1728;
constexpr unsigned WS_E = 0;
constexpr unsigned WS_S1 = 256;
constexpr unsigned WS_ML = WS_S1 + 59u * 4 * 4 * 128 * 8;
constexpr unsigned WS_FG = WS_ML + 4u * 32 * 16 * 2 * 8;
constexpr unsigned WS_END = WS_FG + 4u * 64 * 640 * 8;
constexpr int SPIN_CAP = 1 << 20;

#define SB() __builtin_amdgcn_sched_barrier(0)
#define WG_BARRIER() do { SB(); asm volatile("s_waitcnt lgkmcnt(0)\n\ts_barrier" ::: "memory"); SB(); } while (0)

#define POLL(COND, RELOAD) do { if (!__all(COND)) { int spin = 0; do { __builtin_amdgcn_s_sleep(1); asm volatile("" ::: "memory"); RELOAD; } \
                                  while (!__all(COND) && ++spin < SPIN_CAP); } } while (0)
#define OK2(G) ((((G)[0] & 0xffu) == tb_lo) & ((G)[1] == tb_hi) & (((G)[2] & 0xffu) == tb_lo) & ((G)[3] == tb_hi))

__device__ __forceinline__ u32x4 ldnt(const void* p) { return __builtin_nontemporal_load(reinterpret_cast<const u32x4*>(p)); }
template <int CTRL> __device__ __forceinline__ unsigned dppu(unsigned x) { return (unsigned)__builtin_amdgcn_mov_dpp((int)x, CTRL, 0xf, 0xf, true); }
template <int CTRL> __device__ __forceinline__ float dppf(float x) { return __uint_as_float(dppu<CTRL>(__float_as_uint(x))); }
constexpr int XOR1 = 0xB1, XOR2 = 0x4E, XOR7 = 0x141, ROR8 = 0x128, ROR4 = 0x124, BCAST = 0x150;
__device__ __forceinline__ float dot2(unsigned a, unsigned b, float c) {
  return __builtin_amdgcn_fdot2_f32_bf16(__builtin_bit_cast(bf16x2, a), __builtin_bit_cast(bf16x2, b), c, false);
}
__device__ __forceinline__ unsigned pk_bf16(float lo, float hi) {
  bf16x2 v = {(__bf16)lo, (__bf16)hi};
  return __builtin_bit_cast(unsigned, v);
}
__device__ __forceinline__ unsigned split_hi_lo(float v) {
  const unsigned h = pk_bf16(v, 0.f) & 0xffffu;
  return pk_bf16(v, v - __uint_as_float(h << 16));
}
__device__ __forceinline__ float xrow_sum(float x) {
  auto s = __builtin_amdgcn_permlane16_swap(__float_as_uint(x), __float_as_uint(x), false, false);
  x = __uint_as_float(s[0]) + __uint_as_float(s[1]);
  auto t = __builtin_amdgcn_permlane32_swap(__float_as_uint(x), __float_as_uint(x), false, false);
  return __uint_as_float(t[0]) + __uint_as_float(t[1]);
}
__device__ __forceinline__ float xrow_max(float x) {
  auto s = __builtin_amdgcn_permlane16_swap(__float_as_uint(x), __float_as_uint(x), false, false);
  x = fmaxf(__uint_as_float(s[0]), __uint_as_float(s[1]));
  auto t = __builtin_amdgcn_permlane32_swap(__float_as_uint(x), __float_as_uint(x), false, false);
  return fmaxf(__uint_as_float(t[0]), __uint_as_float(t[1]));
}
__device__ __forceinline__ float row_sum(float x) { x += dppf<XOR1>(x); x += dppf<XOR2>(x); x += dppf<XOR7>(x); x += dppf<ROR8>(x); return x; }
__device__ __forceinline__ float row_max(float x) { x = fmaxf(x, dppf<XOR1>(x)); x = fmaxf(x, dppf<XOR2>(x)); x = fmaxf(x, dppf<XOR7>(x)); x = fmaxf(x, dppf<ROR8>(x)); return x; }

struct Sel { unsigned s[4]; };
__device__ __forceinline__ bf16x8 diagA(unsigned v, const Sel& sel) {
  u32x4 a = {__builtin_amdgcn_perm(v, v, sel.s[0]), __builtin_amdgcn_perm(v, v, sel.s[1]),
             __builtin_amdgcn_perm(v, v, sel.s[2]), __builtin_amdgcn_perm(v, v, sel.s[3])};
  return __builtin_bit_cast(bf16x8, a);
}
__device__ __forceinline__ f32x4 mfma(bf16x8 a, u32x4 b, f32x4 c) {
  return __builtin_amdgcn_mfma_f32_16x16x32_bf16(a, __builtin_bit_cast(bf16x8, b), c, 0, 0, 0);
}

__global__ __launch_bounds__(512, 2) void fused_kernel(
    const u16* __restrict__ x, const u16* __restrict__ kc, const u16* __restrict__ vc, const u16* __restrict__ wq,
    const u16* __restrict__ wo, const u16* __restrict__ wg, const u16* __restrict__ wu, const u16* __restrict__ wd,
    const u16* __restrict__ gw, u16* __restrict__ out, unsigned char* ws) {
  __shared__ __attribute__((aligned(16))) float s1buf[8 * 512];
  __shared__ __attribute__((aligned(16))) float pbuf[8 * 640];
  __shared__ __attribute__((aligned(16))) float obuf[16 * 132];
  __shared__ __attribute__((aligned(16))) u16 xs[8 * 80];
  __shared__ __attribute__((aligned(16))) unsigned qs[64 + 4];

  const int blk = blockIdx.x, tq = blk >> 3;
  const int h = (blk & 7) + 8 * (tq >> 3), b = (tq >> 1) & 3, half = tq & 1, j = 2 * h + half;
  const int tid = threadIdx.x, lane = tid & 63, R = lane >> 4, c = lane & 15;
  const int w = __builtin_amdgcn_readfirstlane(tid >> 6);
  const __amdgpu_buffer_rsrc_t rs = __builtin_amdgcn_make_buffer_rsrc(ws, 0, WS_END, 0x00020000);
  const bool has_s1 = (half == 0) || (h < 27);

  const u32x2 ev = __builtin_amdgcn_raw_buffer_load_b64(rs, WS_E + 8 * ((lane & 1) ? (w & 3) : b), 0, 16);
  const int row0 = 160 * b + 20 * w;
  const int l40 = lane < 40 ? lane : 39;
  const unsigned xv = *reinterpret_cast<const unsigned*>(x + (l40 / 10) * DM + row0 + 2 * (l40 % 10));
  u32x4 gx0, gx1, gg0, gg1;
  if (w == 7) {
    gx0 = *reinterpret_cast<const u32x4*>(x + b * DM + 16 * l40); gx1 = *reinterpret_cast<const u32x4*>(x + b * DM + 16 * l40 + 8);
    gg0 = *reinterpret_cast<const u32x4*>(gw + 16 * l40);          gg1 = *reinterpret_cast<const u32x4*>(gw + 16 * l40 + 8);
  }
  u32x4 s1[5];
  {
    const int ld = half ? DF : NH * DH, hc = h < 27 ? h : h - 8;
    const u16* p = half ? ((c < 8 ? wg : wu) + 64 * hc + 8 * (c & 7)) : (wq + DH * h + 8 * c);
    p += (row0 + R) * ld;
#pragma unroll
    for (int i = 0; i < 5; ++i) s1[i] = ldnt(p + 4 * i * ld);
  }
  SB();
  const size_t kvoff = ((size_t)(b * NH + h) * SK + (SK - WIN) + half * 512 + 64 * w) * DH;
  const u16* kp = kc + kvoff + 8 * lane;
  const u16* vp = vc + kvoff + 8 * lane;
  const u16* wop = wo + (size_t)(h * DH + 16 * w + R) * DM + 8 * c;
  u32x4 kr[16], vr[16], wr[20], dr[5];
#pragma unroll
  for (int i = 0; i < 8; ++i) kr[i] = ldnt(kp + 512 * i);
  SB();

  Sel sel;
  {
    const unsigned b0 = 2 * (c >> 3), pr = b0 | ((b0 + 1) << 8), e = c & 7;
    const unsigned on = (e & 1) ? ((pr << 16) | 0x0C0Cu) : (0x0C0C0000u | pr);
#pragma unroll
    for (int q = 0; q < 4; ++q) sel.s[q] = ((e >> 1) == (unsigned)q) ? on : 0x0C0C0C0Cu;
  }
  if (lane < 40) {
    const int bb = lane / 10, rr = 2 * (lane % 10);
    xs[w * 80 + rr * 4 + bb] = (u16)xv; xs[w * 80 + rr * 4 + 4 + bb] = (u16)(xv >> 16);
  }
  if (w == 7) {
    float a = 0.f;
#pragma unroll
    for (int q = 0; q < 4; ++q) { a = dot2(gx0[q], gg0[q], a); a = dot2(gx1[q], gg1[q], a); }
    a = lane < 40 ? a : 0.f;
    a = xrow_sum(row_sum(a));
    if (lane == 0) qs[64] = __float_as_uint(1.f / (1.f + __expf(-a)));
  }
  {
    f32x4 d0 = {0.f, 0.f, 0.f, 0.f}, d1 = d0;
#pragma unroll
    for (int i = 0; i < 5; ++i) {
      const u32x2 xx = *reinterpret_cast<const u32x2*>(&xs[w * 80 + (4 * i + R) * 4]);
      d0 = mfma(diagA(xx[0], sel), s1[i], d0);
      d1 = mfma(diagA(xx[1], sel), s1[i], d1);
    }
    float* dst = &s1buf[w * 512 + (R >> 1) * 128 + 8 * c + 4 * (R & 1)];
    *reinterpret_cast<f32x4*>(dst) = d0; *reinterpret_cast<f32x4*>(dst + 256) = d1;
  }
  const unsigned long long tb = (((unsigned long long)__builtin_amdgcn_readlane(ev[1], 0) << 32) | (unsigned)__builtin_amdgcn_readlane(ev[0], 0)) + 1;
  const unsigned long long tw = (((unsigned long long)__builtin_amdgcn_readlane(ev[1], 1) << 32) | (unsigned)__builtin_amdgcn_readlane(ev[0], 1)) + 1;
  const unsigned tb_lo = (unsigned)tb & 0xffu, tb_hi = (unsigned)(tb >> 8), tb32 = (unsigned)tb;
  WG_BARRIER();
  if (has_s1 && w < 4) {
    f32x2 a = {0.f, 0.f};
#pragma unroll
    for (int q = 0; q < 8; ++q) a += *reinterpret_cast<const f32x2*>(&s1buf[q * 512 + w * 128 + 2 * lane]);
    const unsigned lo = (unsigned)tw & 0xffu, hi = (unsigned)(tw >> 8);
    const u32x4 g = {(__float_as_uint(a[0]) & ~0xffu) | lo, hi, (__float_as_uint(a[1]) & ~0xffu) | lo, hi};
    const int tg = half ? 32 + h : h;
    __builtin_amdgcn_raw_buffer_store_b128(g, rs, WS_S1 + (((tg * 4 + w) * 4 + b) * 128 + 2 * lane) * 8, 0, 16);
  }
  SB();
#pragma unroll
  for (int i = 8; i < 16; ++i) kr[i] = ldnt(kp + 512 * i);
  SB();
  const unsigned qoff = WS_S1 + (((h * 4 + b) * 4 + (lane & 3)) * 128 + 16 * w + 2 * ((lane >> 2) & 7)) * 8;
  u32x4 qg = __builtin_amdgcn_raw_buffer_load_b128(rs, qoff, 0, 16);
  SB();
#pragma unroll
  for (int i = 0; i < 8; ++i) vr[i] = ldnt(vp + 512 * i);
  SB();
  POLL(OK2(qg), qg = __builtin_amdgcn_raw_buffer_load_b128(rs, qoff, 0, 16));
  {
    float q0 = __uint_as_float(qg[0] & ~0xffu), q1 = __uint_as_float(qg[2] & ~0xffu);
    q0 += dppf<XOR1>(q0); q1 += dppf<XOR1>(q1);
    q0 += dppf<XOR2>(q0); q1 += dppf<XOR2>(q1);
    if ((lane & 3) == 0 && lane < 32) qs[8 * w + (lane >> 2)] = pk_bf16(q0, q1);
  }
  WG_BARRIER();
  const u32x4 q4 = *reinterpret_cast<const u32x4*>(&qs[4 * c]);
  const float gate = __uint_as_float(qs[64]);
  float s[16];
#pragma unroll
  for (int i = 0; i < 16; ++i) {
    float a = dot2(kr[i][0], q4[0], 0.f);
    a = dot2(kr[i][1], q4[1], a); a = dot2(kr[i][2], q4[2], a); s[i] = dot2(kr[i][3], q4[3], a);
  }
  {
    const bool b8 = c & 8, b4 = c & 4, b2 = c & 2, b1 = c & 1;
#pragma unroll
    for (int i = 0; i < 8; ++i) s[i] = (b8 ? s[i + 8] : s[i]) + dppf<ROR8>(b8 ? s[i] : s[i + 8]);
#pragma unroll
    for (int i = 0; i < 4; ++i) s[i] = (b4 ? s[i + 4] : s[i]) + dppf<XOR7>(b4 ? s[i] : s[i + 4]);
#pragma unroll
    for (int i = 0; i < 2; ++i) s[i] = (b2 ? s[i + 2] : s[i]) + dppf<XOR2>(b2 ? s[i] : s[i + 2]);
    s[0] = (b1 ? s[1] : s[0]) + dppf<XOR1>(b1 ? s[0] : s[1]);
  }
  const float sc = s[0] * (0.08838834764831845f * 1.4426950408889634f);
  const float mw = xrow_max(row_max(sc));
  const float p = __builtin_amdgcn_exp2f(sc - mw);
  const float lw = xrow_sum(row_sum(p));
  const unsigned mlbase = WS_ML + (b * NH + h) * 256;
  if (lane == 0) {
    const u32x4 g = {__float_as_uint(mw), tb32, __float_as_uint(lw), tb32};
    __builtin_amdgcn_raw_buffer_store_b128(g, rs, mlbase + (half * 8 + w) * 16, 0, 16);
  }
  const unsigned pp = split_hi_lo(p);
  SB();
  const int fl = 4 * w + R, f = 27 * j + (fl < 27 ? fl : 26);
  const unsigned hoff = WS_S1 + ((((32 + (f >> 6)) * 4 + b) * 4 + (c & 3)) * 128 + ((c >> 2) & 1) * 64 + (f & 63)) * 8;
  u32x2 hg;
  if (w < 7) {
    hg = __builtin_amdgcn_raw_buffer_load_b64(rs, hoff, 0, 16);
#pragma unroll
    for (int G = 0; G < 5; ++G) dr[G] = ldnt(wd + (size_t)f * DM + 128 * G + 8 * c);
  }
  SB();
#pragma unroll
  for (int i = 0; i < 8; ++i) {
    wr[2 * i] = ldnt(wop + (2 * i / 5) * 4 * DM + 128 * (2 * i % 5));
    wr[2 * i + 1] = ldnt(wop + ((2 * i + 1) / 5) * 4 * DM + 128 * ((2 * i + 1) % 5));
    if (i < 4) wr[16 + i] = ldnt(wop + 3 * 4 * DM + 128 * (1 + i));
    vr[8 + i] = ldnt(vp + 512 * (8 + i));
    SB();
  }
  u32x4 mg = __builtin_amdgcn_raw_buffer_load_b128(rs, mlbase + c * 16, 0, 16);
  SB();
  f32x4 o0 = {0.f, 0.f, 0.f, 0.f}, o1 = o0;
#define PV_STEP(I, ACC) ACC = mfma(diagA(dppu<BCAST + (I)>(pp), sel), vr[I], ACC)
  PV_STEP(0, o0); PV_STEP(1, o1); PV_STEP(2, o0); PV_STEP(3, o1); PV_STEP(4, o0); PV_STEP(5, o1); PV_STEP(6, o0); PV_STEP(7, o1);
  f32x4 acc[5];
#pragma unroll
  for (int G = 0; G < 5; ++G) acc[G] = f32x4{0.f, 0.f, 0.f, 0.f};
  if (w < 7) {
    POLL(((hg[0] & 0xffu) == tb_lo) & (hg[1] == tb_hi), hg = __builtin_amdgcn_raw_buffer_load_b64(rs, hoff, 0, 16));
    float a = __uint_as_float(hg[0] & ~0xffu);
    a += dppf<XOR1>(a); a += dppf<XOR2>(a);
    const float gv = dppf<BCAST + 0>(a), uv = dppf<BCAST + 4>(a);
    const float z2 = 1.5957691216057308f * (gv + 0.044715f * gv * gv * gv);
    float hv = gv / (1.f + __expf(-z2)) * uv * (1.f - gate);
    hv = fl < 27 ? hv : 0.f;
    const bf16x8 A = diagA(split_hi_lo(hv), sel);
#pragma unroll
    for (int G = 0; G < 5; ++G) acc[G] = mfma(A, dr[G], acc[G]);
  }
  PV_STEP(8, o0); PV_STEP(9, o1); PV_STEP(10, o0); PV_STEP(11, o1); PV_STEP(12, o0); PV_STEP(13, o1); PV_STEP(14, o0); PV_STEP(15, o1);
  POLL((mg[1] == tb32) & (mg[3] == tb32), mg = __builtin_amdgcn_raw_buffer_load_b128(rs, mlbase + c * 16, 0, 16));
  {
    const float mi = __uint_as_float(mg[0]), li = __uint_as_float(mg[2]);
    const float M = row_max(mi);
    const float L = row_sum(li * __builtin_amdgcn_exp2f(mi - M));
    const float scale = __builtin_amdgcn_exp2f(mw - M) / L * gate;
    o0 = (o0 + o1) * scale;
    *reinterpret_cast<f32x4*>(&obuf[(2 * w + (R >> 1)) * 132 + 8 * c + 4 * (R & 1)]) = o0;
  }
  WG_BARRIER();
  {
    const float* op = &obuf[(c >> 2) * 4 * 132 + 16 * w + 4 * (c & 3) + R];
    float o = (op[0] + op[132]) + (op[264] + op[396]);
    o += dppf<ROR4>(o); o += dppf<ROR8>(o);
    const unsigned ov = split_hi_lo(o);
#define WO_STEP(RG) { const bf16x8 A = diagA(dppu<(RG) * 0x55>(ov), sel); \
    _Pragma("unroll") for (int G = 0; G < 5; ++G) acc[G] = mfma(A, wr[5 * (RG) + G], acc[G]); }
    WO_STEP(0) WO_STEP(1) WO_STEP(2) WO_STEP(3)
#pragma unroll
    for (int G = 0; G < 5; ++G) {
      auto u = __builtin_amdgcn_permlane32_swap(__float_as_uint(acc[G][0]), __float_as_uint(acc[G][2]), false, false);
      auto v = __builtin_amdgcn_permlane32_swap(__float_as_uint(acc[G][1]), __float_as_uint(acc[G][3]), false, false);
      const f32x2 r = {__uint_as_float(u[0]) + __uint_as_float(u[1]), __uint_as_float(v[0]) + __uint_as_float(v[1])};
      *reinterpret_cast<f32x2*>(&pbuf[w * 640 + 128 * G + 8 * c + 4 * (R & 1) + 2 * (R >> 1)]) = r;
    }
  }
  WG_BARRIER();
  if (w < 5) {
    f32x2 a = {0.f, 0.f};
#pragma unroll
    for (int q = 0; q < 8; ++q) a += *reinterpret_cast<const f32x2*>(&pbuf[q * 640 + 2 * tid]);
    const u32x4 g = {(__float_as_uint(a[0]) & ~0xffu) | tb_lo, tb_hi, (__float_as_uint(a[1]) & ~0xffu) | tb_lo, tb_hi};
    __builtin_amdgcn_raw_buffer_store_b128(g, rs, WS_FG + ((b * 64 + j) * 640 + 2 * tid) * 8, 0, 16);
  } else if (w == 7 && j < 40) {
    const unsigned foff = WS_FG + ((b * 64 + (lane >> 3)) * 640 + 16 * j + 2 * (lane & 7)) * 8;
    u32x4 fg[8];
    bool ok[8];
#pragma unroll
    for (int k = 0; k < 8; ++k) ok[k] = false;
    for (int spin = 0; spin < SPIN_CAP; ++spin) {
#pragma unroll
      for (int k = 0; k < 8; ++k) if (!ok[k]) fg[k] = __builtin_amdgcn_raw_buffer_load_b128(rs, foff, k * 8 * 640 * 8, 16);
      bool all = true;
#pragma unroll
      for (int k = 0; k < 8; ++k) {
        ok[k] = OK2(fg[k]);
        all &= ok[k];
      }
      if (__all(all)) break;
      __builtin_amdgcn_s_sleep(1);
      asm volatile("" ::: "memory");
    }
    float a0 = 0.f, a1 = 0.f;
#pragma unroll
    for (int k = 0; k < 8; ++k) { a0 += __uint_as_float(fg[k][0] & ~0xffu); a1 += __uint_as_float(fg[k][2] & ~0xffu); }
    a0 += dppf<ROR8>(a0); a1 += dppf<ROR8>(a1);
    a0 = xrow_sum(a0); a1 = xrow_sum(a1);
    if (lane < 8) *reinterpret_cast<unsigned*>(out + b * DM + 16 * j + 2 * lane) = pk_bf16(a0, a1);
    if (j == 0 && lane == 0) {
      const u32x2 e = {(unsigned)tb, (unsigned)(tb >> 32)};
      __builtin_amdgcn_raw_buffer_store_b64(e, rs, WS_E + 8 * b, 0, 16);
    }
  }
}

extern "C" void kernel_launch(void* const* d_in, const int* in_sizes, int n_in, void* d_out, int out_size, void* d_ws,
                              size_t ws_size, hipStream_t stream) {
  fused_kernel<<<256, 512, 0, stream>>>((const u16*)d_in[0], (const u16*)d_in[1], (const u16*)d_in[2], (const u16*)d_in[3],
                                        (const u16*)d_in[4], (const u16*)d_in[6], (const u16*)d_in[7], (const u16*)d_in[8],
                                        (const u16*)d_in[9], (u16*)d_out, (unsigned char*)d_ws);
}
```

```cpp
#include <hip/hip_runtime.h>
#include <hip/hip_bf16.h>
#include <stdint.h>

typedef __attribute__((ext_vector_type(4))) unsigned u32x4;
typedef __attribute__((ext_vector_type(2))) unsigned u32x2;
typedef __attribute__((ext_vector_type(4))) float f32x4;
typedef __attribute__((ext_vector_type(2))) float f32x2;
typedef __attribute__((ext_vector_type(8))) __bf16 bf16x8;
typedef __attribute__((ext_vector_type(2))) __bf16 bf16x2;
typedef unsigned short u16;

constexpr int DM = 640, NH = 32, SK = 4096, WIN = 1024, DH = 128, DF = 1728;
constexpr unsigned WS_E = 0;
constexpr unsigned WS_S1 = 256;
constexpr unsigned WS_ML = WS_S1 + 59u * 4 * 4 * 128 * 8;
constexpr unsigned WS_FG = WS_ML + 4u * 32 * 16 * 2 * 8;
constexpr unsigned WS_END = WS_FG + 4u * 64 * 640 * 8;
constexpr int SPIN_CAP = 1 << 20;

#define SB() __builtin_amdgcn_sched_barrier(0)
#define ST(i) do { } while (0)
#define RETRY(i) 0
#define WG_BARRIER() do { SB(); asm volatile("s_waitcnt lgkmcnt(0)\n\ts_barrier" ::: "memory"); SB(); } while (0)

#define LDK(i) do { kr[i] = ldnt(kp + 512 * (i)); SB(); } while (0)
#define LDV(i) do { vr[i] = ldnt(vp + 512 * (i)); SB(); } while (0)
#define WO_AUX 2
#define LDW(i) do { wr[i] = __builtin_amdgcn_raw_buffer_load_b128(rwo, wooff + 256 * ((i) % 5), ((i) / 5) * 4 * DM * 2, WO_AUX); SB(); } while (0)
#define LDS_WAIT(PTR, VAL) do { int spin = 0; while (__hip_atomic_load(PTR, __ATOMIC_RELAXED, __HIP_MEMORY_SCOPE_WORKGROUP) < (VAL) && ++spin < SPIN_CAP) \
                                  __builtin_amdgcn_s_sleep(0); asm volatile("" ::: "memory"); SB(); } while (0)
#define LDS_DRAIN() do { SB(); asm volatile("s_waitcnt lgkmcnt(0)" ::: "memory"); SB(); } while (0)
#define POLL(ID, COND, RELOAD) do { if (!__all(COND)) { int spin = 0; do { (void)RETRY(ID); __builtin_amdgcn_s_sleep(1); asm volatile("" ::: "memory"); RELOAD; } \
                                  while (!__all(COND) && ++spin < SPIN_CAP); } } while (0)
#define OK2(G) ((((G)[0] & 0xffu) == tb_lo) & ((G)[1] == tb_hi) & (((G)[2] & 0xffu) == tb_lo) & ((G)[3] == tb_hi))

__device__ __forceinline__ u32x4 ldnt(const void* p) { return __builtin_nontemporal_load(reinterpret_cast<const u32x4*>(p)); }
template <int CTRL> __device__ __forceinline__ unsigned dppu(unsigned x) { return (unsigned)__builtin_amdgcn_mov_dpp((int)x, CTRL, 0xf, 0xf, true); }
template <int CTRL> __device__ __forceinline__ float dppf(float x) { return __uint_as_float(dppu<CTRL>(__float_as_uint(x))); }
constexpr int XOR1 = 0xB1, XOR2 = 0x4E, XOR7 = 0x141, ROR8 = 0x128, ROR4 = 0x124, BCAST = 0x150;
__device__ __forceinline__ float dot2(unsigned a, unsigned b, float c) {
  return __builtin_amdgcn_fdot2_f32_bf16(__builtin_bit_cast(bf16x2, a), __builtin_bit_cast(bf16x2, b), c, false);
}
__device__ __forceinline__ unsigned pk_bf16(float lo, float hi) {
  bf16x2 v = {(__bf16)lo, (__bf16)hi};
  return __builtin_bit_cast(unsigned, v);
}
__device__ __forceinline__ unsigned split_hi_lo(float v) {
  const unsigned h = pk_bf16(v, 0.f) & 0xffffu;
  return pk_bf16(v, v - __uint_as_float(h << 16));
}
__device__ __forceinline__ float xrow_sum(float x) {
  auto s = __builtin_amdgcn_permlane16_swap(__float_as_uint(x), __float_as_uint(x), false, false);
  x = __uint_as_float(s[0]) + __uint_as_float(s[1]);
  auto t = __builtin_amdgcn_permlane32_swap(__float_as_uint(x), __float_as_uint(x), false, false);
  return __uint_as_float(t[0]) + __uint_as_float(t[1]);
}
__device__ __forceinline__ float xrow_max(float x) {
  auto s = __builtin_amdgcn_permlane16_swap(__float_as_uint(x), __float_as_uint(x), false, false);
  x = fmaxf(__uint_as_float(s[0]), __uint_as_float(s[1]));
  auto t = __builtin_amdgcn_permlane32_swap(__float_as_uint(x), __float_as_uint(x), false, false);
  return fmaxf(__uint_as_float(t[0]), __uint_as_float(t[1]));
}
__device__ __forceinline__ float row_sum(float x) { x += dppf<XOR1>(x); x += dppf<XOR2>(x); x += dppf<XOR7>(x); x += dppf<ROR8>(x); return x; }
__device__ __forceinline__ float row_max(float x) { x = fmaxf(x, dppf<XOR1>(x)); x = fmaxf(x, dppf<XOR2>(x)); x = fmaxf(x, dppf<XOR7>(x)); x = fmaxf(x, dppf<ROR8>(x)); return x; }

struct Sel { unsigned s[4]; };
__device__ __forceinline__ bf16x8 diagA(unsigned v, const Sel& sel) {
  u32x4 a = {__builtin_amdgcn_perm(v, v, sel.s[0]), __builtin_amdgcn_perm(v, v, sel.s[1]),
             __builtin_amdgcn_perm(v, v, sel.s[2]), __builtin_amdgcn_perm(v, v, sel.s[3])};
  return __builtin_bit_cast(bf16x8, a);
}
__device__ __forceinline__ f32x4 mfma(bf16x8 a, u32x4 b, f32x4 c) {
  return __builtin_amdgcn_mfma_f32_16x16x32_bf16(a, __builtin_bit_cast(bf16x8, b), c, 0, 0, 0);
}

__global__ __launch_bounds__(512, 2) void fused_kernel(
    const u16* __restrict__ x, const u16* __restrict__ kc, const u16* __restrict__ vc, const u16* __restrict__ wq,
    const u16* __restrict__ wo, const u16* __restrict__ wg, const u16* __restrict__ wu, const u16* __restrict__ wd,
    const u16* __restrict__ gw, u16* __restrict__ out, unsigned char* ws) {
  __shared__ __attribute__((aligned(16))) float s1buf[4 * 512];
  __shared__ __attribute__((aligned(16))) float pbuf[8 * 640];
  __shared__ __attribute__((aligned(16))) float obuf[8 * 132];
  __shared__ __attribute__((aligned(16))) u16 xs[4 * 160];
  __shared__ __attribute__((aligned(16))) unsigned ps[4 * 128];
  __shared__ __attribute__((aligned(16))) unsigned flg[16];

  ST(0);
  const int blk = blockIdx.x, tq = blk >> 3;

  const int h = (blk & 7) + 8 * (tq >> 3),
             b = (tq >> 1) & 3, half = tq & 1, j = 2 * h + half;
  const int tid = threadIdx.x, lane = tid & 63, R = lane >> 4, c = lane & 15;
  const int w = __builtin_amdgcn_readfirstlane(tid >> 6), w4 = w & 3;
  const __amdgpu_buffer_rsrc_t rs = __builtin_amdgcn_make_buffer_rsrc(ws, 0, WS_END, 0x00020000);
  const size_t kvoff = ((size_t)(b * NH + h) * SK + (SK - WIN) + half * 512 + 128 * w4) * DH;
  const __amdgpu_buffer_rsrc_t rwo = __builtin_amdgcn_make_buffer_rsrc((void*)(wo + (size_t)(h * DH + 16 * w) * DM), 0, 16 * DM * 2, 0x00020000);
  const unsigned wooff = (R * DM + 8 * c) * 2;
  const u32x2 ev = __builtin_amdgcn_raw_buffer_load_b64(rs, WS_E + 8 * ((lane & 1) ? w4 : b), 0, 16);
  u32x4 wr[20];
  f32x4 acc[5];
#pragma unroll
  for (int G = 0; G < 5; ++G) acc[G] = f32x4{0.f, 0.f, 0.f, 0.f};
  Sel sel;
  unsigned long long tb;
  unsigned tb_lo, tb_hi;
#define MAKE_SEL() do { const unsigned b0 = 2 * (c >> 3), pr = b0 | ((b0 + 1) << 8), e = c & 7; \
    const unsigned on = (e & 1) ? ((pr << 16) | 0x0C0Cu) : (0x0C0C0000u | pr); \
    _Pragma("unroll") for (int q = 0; q < 4; ++q) sel.s[q] = ((e >> 1) == (unsigned)q) ? on : 0x0C0C0C0Cu; } while (0)
#define MAKE_TAG() do { tb = (((unsigned long long)__builtin_amdgcn_readlane(ev[1], 0) << 32) | (unsigned)__builtin_amdgcn_readlane(ev[0], 0)) + 1; \
    tb_lo = (unsigned)tb & 0xffu; tb_hi = (unsigned)(tb >> 8); } while (0)

  if (w < 4) {
    __builtin_amdgcn_s_setprio(2);
    const bool has_s1 = (half == 0) || (h < 27);
    const int row0 = 160 * b + 40 * w;
    const int l40 = lane < 40 ? lane : 39;
    const u32x2 xv = *reinterpret_cast<const u32x2*>(x + (l40 / 10) * DM + row0 + 4 * (l40 % 10));
    u32x4 s1[10], kr[32];
    {
      const int ld = half ? DF : NH * DH, hc = h < 27 ? h : 26;
      const u16* p = half ? ((c < 8 ? wg : wu) + 64 * hc + 8 * (c & 7)) : (wq + DH * h + 8 * c);
      p += (row0 + R) * ld;
#pragma unroll
      for (int i = 0; i < 10; ++i) s1[i] = ldnt(p + 4 * i * ld);
    }
    const u32x4 gx0 = *reinterpret_cast<const u32x4*>(x + b * DM + 16 * l40), gx1 = *reinterpret_cast<const u32x4*>(x + b * DM + 16 * l40 + 8);
    const u32x4 gg0 = *reinterpret_cast<const u32x4*>(gw + 16 * l40), gg1 = *reinterpret_cast<const u32x4*>(gw + 16 * l40 + 8);
    if (tid < 16) flg[tid] = 0;
    WG_BARRIER();
    const u16* kp = kc + kvoff + 8 * lane;
    LDK(0); LDK(1); LDK(2); LDK(3);
    MAKE_SEL();
    if (lane < 40) {
      u16* d = &xs[w * 160 + (lane % 10) * 16 + lane / 10];
      d[0] = (u16)xv[0]; d[4] = (u16)(xv[0] >> 16); d[8] = (u16)xv[1]; d[12] = (u16)(xv[1] >> 16);
    }
    {
      f32x4 d0 = {0.f, 0.f, 0.f, 0.f}, d1 = d0;
#pragma unroll
      for (int i = 0; i < 10; ++i) {
        const u32x2 xx = *reinterpret_cast<const u32x2*>(&xs[w * 160 + (4 * i + R) * 4]);
        d0 = mfma(diagA(xx[0], sel), s1[i], d0); d1 = mfma(diagA(xx[1], sel), s1[i], d1);
      }
      float* dst = &s1buf[w * 512 + (R >> 1) * 128 + 8 * c + 4 * (R & 1)];
      *reinterpret_cast<f32x4*>(dst) = d0; *reinterpret_cast<f32x4*>(dst + 256) = d1;
    }
    LDS_DRAIN();
    if (lane == 0) __hip_atomic_fetch_add(&flg[0], 1u, __ATOMIC_RELAXED, __HIP_MEMORY_SCOPE_WORKGROUP);
    ST(1);
    MAKE_TAG();
    const unsigned long long tw = (((unsigned long long)__builtin_amdgcn_readlane(ev[1], 1) << 32) | (unsigned)__builtin_amdgcn_readlane(ev[0], 1)) + 1;
    float gate;
    {
      float a = 0.f;
#pragma unroll
      for (int q = 0; q < 4; ++q) { a = dot2(gx0[q], gg0[q], a); a = dot2(gx1[q], gg1[q], a); }
      a = lane < 40 ? a : 0.f;
      a = xrow_sum(row_sum(a));
      gate = 1.f / (1.f + __expf(-a));
    }
    LDS_WAIT(&flg[0], 4u);
    if (has_s1) {
      f32x2 a = {0.f, 0.f};
#pragma unroll
      for (int q = 0; q < 4; ++q) a += *reinterpret_cast<const f32x2*>(&s1buf[q * 512 + w * 128 + 2 * lane]);
      const unsigned lo = (unsigned)tw & 0xffu, hi = (unsigned)(tw >> 8);
      const u32x4 g = {(__float_as_uint(a[0]) & ~0xffu) | lo, hi, (__float_as_uint(a[1]) & ~0xffu) | lo, hi};
      const int tg = half ? 32 + h : h;
      __builtin_amdgcn_raw_buffer_store_b128(g, rs, WS_S1 + (tg * 4 + w) * 4096 + ((lane & 3) * 64 + b * 16 + (lane >> 2)) * 16, 0, 16);
    }
    SB();
    ST(2);
#pragma unroll
    for (int i = 0; i < 6; ++i) { LDK(4 + 3 * i); LDK(5 + 3 * i); LDK(6 + 3 * i); LDW(i); }
    const unsigned qoff = WS_S1 + (h * 4 + b) * 4096 + lane * 16;
    u32x4 qg[4];
#define LDQ() do { _Pragma("unroll") for (int q = 0; q < 4; ++q) qg[q] = __builtin_amdgcn_raw_buffer_load_b128(rs, qoff + 1024 * q, 0, 16); } while (0)
    LDQ();
    SB();
    LDK(22); LDK(23); LDK(24); LDK(25); LDK(26); LDW(6); LDK(27); LDK(28); LDK(29); LDK(30); LDK(31); LDW(7);
    POLL(0, OK2(qg[0]) & OK2(qg[1]) & OK2(qg[2]) & OK2(qg[3]), LDQ());
    ST(3);
    u32x4 q4;
#pragma unroll
    for (int q = 0; q < 4; ++q) q4[q] = pk_bf16(xrow_sum(__uint_as_float(qg[q][0] & ~0xffu)), xrow_sum(__uint_as_float(qg[q][2] & ~0xffu)));
    ST(4);
    float sc[2];
    const bool b8 = c & 8, b4 = c & 4, b2 = c & 2, b1 = c & 1;
#pragma unroll
    for (int t = 0; t < 2; ++t) {
      float s[16];
#pragma unroll
      for (int i = 0; i < 16; ++i) {
        float a = dot2(kr[16 * t + i][0], q4[0], 0.f);
        a = dot2(kr[16 * t + i][1], q4[1], a); a = dot2(kr[16 * t + i][2], q4[2], a); s[i] = dot2(kr[16 * t + i][3], q4[3], a);
      }
#pragma unroll
      for (int i = 0; i < 8; ++i) s[i] = (b8 ? s[i + 8] : s[i]) + dppf<ROR8>(b8 ? s[i] : s[i + 8]);
#pragma unroll
      for (int i = 0; i < 4; ++i) s[i] = (b4 ? s[i + 4] : s[i]) + dppf<XOR7>(b4 ? s[i] : s[i + 4]);
#pragma unroll
      for (int i = 0; i < 2; ++i) s[i] = (b2 ? s[i + 2] : s[i]) + dppf<XOR2>(b2 ? s[i] : s[i + 2]);
      sc[t] = ((b1 ? s[1] : s[0]) + dppf<XOR1>(b1 ? s[0] : s[1])) * (0.08838834764831845f * 1.4426950408889634f);
      SB();
    }
    const float mw = xrow_max(row_max(fmaxf(sc[0], sc[1])));
    const float p0 = __builtin_amdgcn_exp2f(sc[0] - mw), p1 = __builtin_amdgcn_exp2f(sc[1] - mw);
    *reinterpret_cast<u32x2*>(&ps[w * 128 + 2 * lane]) = u32x2{split_hi_lo(p0), split_hi_lo(p1)};
    LDS_DRAIN();
    if (lane == 0) __hip_atomic_store(&flg[4 + w], 1u, __ATOMIC_RELAXED, __HIP_MEMORY_SCOPE_WORKGROUP);
    const float lw = xrow_sum(row_sum(p0 + p1));
    const unsigned mlbase = WS_ML + (b * NH + h) * 256, tb32 = (unsigned)tb;
    if (lane == 0) {
      const u32x4 g = {__float_as_uint(mw), tb32, __float_as_uint(lw), tb32};
      __builtin_amdgcn_raw_buffer_store_b128(g, rs, mlbase + (half * 4 + w) * 16, 0, 16);
    }
    SB();
    ST(5);
    const int fl = 8 * w + 4 * (c >> 3) + R, f = 27 * j + (fl < 27 ? fl : 26);
    const int hcol = ((c >> 2) & 1) * 64 + (f & 63);
    const unsigned hoff = WS_S1 + ((32 + (f >> 6)) * 4 + b) * 4096 + (((hcol >> 1) & 3) * 64 + (c & 3) * 16 + (hcol >> 3)) * 16 + (hcol & 1) * 8;
    u32x2 hg = __builtin_amdgcn_raw_buffer_load_b64(rs, hoff, 0, 16);
    u32x4 dr[10];
    {
      const int f0 = 8 * w + R, f1 = f0 + 4;
      const __amdgpu_buffer_rsrc_t rwd = __builtin_amdgcn_make_buffer_rsrc((void*)(wd + (size_t)27 * j * DM), 0, 27 * DM * 2, 0x00020000);
      const unsigned d0 = ((f0 < 27 ? f0 : 26) * DM + 8 * c) * 2, d1 = ((f1 < 27 ? f1 : 26) * DM + 8 * c) * 2;
#pragma unroll
      for (int G = 0; G < 5; ++G) dr[G] = __builtin_amdgcn_raw_buffer_load_b128(rwd, d0 + 256 * G, 0, WO_AUX);
#pragma unroll
      for (int G = 0; G < 5; ++G) dr[5 + G] = __builtin_amdgcn_raw_buffer_load_b128(rwd, d1 + 256 * G, 0, WO_AUX);
    }
    SB();
    LDW(8); LDW(9); LDW(10); LDW(11); LDW(12); LDW(13);
    u32x4 mg = __builtin_amdgcn_raw_buffer_load_b128(rs, mlbase + (c & 7) * 16, 0, 16);
    SB();
    LDW(14); LDW(15); LDW(16); LDW(17); LDW(18); LDW(19);
    ST(6);
    {
      POLL(1, ((hg[0] & 0xffu) == tb_lo) & (hg[1] == tb_hi), hg = __builtin_amdgcn_raw_buffer_load_b64(rs, hoff, 0, 16));
      float a = __uint_as_float(hg[0] & ~0xffu);
      a += dppf<XOR1>(a); a += dppf<XOR2>(a);
      const float other = dppf<XOR7>(a);
      const float gv = (c & 4) ? other : a, uv = (c & 4) ? a : other;
      const float z2 = 1.5957691216057308f * (gv + 0.044715f * gv * gv * gv);
      float hv = gv / (1.f + __expf(-z2)) * uv * (1.f - gate);
      hv = fl < 27 ? hv : 0.f;
      const unsigned hh = split_hi_lo(hv);
      const bf16x8 A0 = diagA(dppu<BCAST + 0>(hh), sel), A1 = diagA(dppu<BCAST + 8>(hh), sel);
#pragma unroll
      for (int G = 0; G < 5; ++G) acc[G] = mfma(A0, dr[G], acc[G]);
#pragma unroll
      for (int G = 0; G < 5; ++G) acc[G] = mfma(A1, dr[5 + G], acc[G]);
    }
    SB();
    POLL(2, (mg[1] == tb32) & (mg[3] == tb32), mg = __builtin_amdgcn_raw_buffer_load_b128(rs, mlbase + (c & 7) * 16, 0, 16));
    {
      const float mi = __uint_as_float(mg[0]), li = __uint_as_float(mg[2]);
      float M = fmaxf(mi, dppf<XOR1>(mi)); M = fmaxf(M, dppf<XOR2>(M)); M = fmaxf(M, dppf<XOR7>(M));
      float L = li * __builtin_amdgcn_exp2f(mi - M);
      L += dppf<XOR1>(L); L += dppf<XOR2>(L); L += dppf<XOR7>(L);
      if (lane == 0) flg[8 + w] = __float_as_uint(__builtin_amdgcn_exp2f(mw - M) / L * gate);
    }
    ST(7);
  } else {
    WG_BARRIER();
    const u16* vp = vc + kvoff + 8 * lane;
    u32x4 vr[32];
#pragma unroll
    for (int i = 0; i < 16; ++i) { LDV(2 * i); LDV(2 * i + 1); LDW(i); }
    LDW(16); LDW(17); LDW(18); LDW(19);
    ST(5);
    MAKE_SEL();
    MAKE_TAG();
    LDS_WAIT(&flg[w], 1u);
    ST(6);
    const u32x2 pp = *reinterpret_cast<const u32x2*>(&ps[w4 * 128 + 2 * lane]);
    f32x4 o0 = {0.f, 0.f, 0.f, 0.f}, o1 = o0;
#define PV2(I) do { o0 = mfma(diagA(dppu<BCAST + ((I) & 15)>(pp[(I) >> 4]), sel), vr[I], o0); \
                    o1 = mfma(diagA(dppu<BCAST + (((I) + 1) & 15)>(pp[(I) >> 4]), sel), vr[(I) + 1], o1); SB(); } while (0)
    PV2(0); PV2(2); PV2(4); PV2(6); PV2(8); PV2(10); PV2(12); PV2(14);
    PV2(16); PV2(18); PV2(20); PV2(22); PV2(24); PV2(26); PV2(28); PV2(30);
    *reinterpret_cast<f32x4*>(&obuf[(2 * w4 + (R >> 1)) * 132 + 8 * c + 4 * (R & 1)]) = o0 + o1;
    ST(7);
  }
  WG_BARRIER();
  ST(8);
  {
    const float* op = &obuf[(c >> 2) * 2 * 132 + 16 * w + 4 * (c & 3) + R];
    float o = (op[0] + op[132]) * __uint_as_float(flg[8 + (c >> 2)]);
    o += dppf<ROR4>(o); o += dppf<ROR8>(o);
    const unsigned ov = split_hi_lo(o);
#define WO_STEP(RG) { const bf16x8 A = diagA(dppu<(RG) * 0x55>(ov), sel); \
    _Pragma("unroll") for (int G = 0; G < 5; ++G) acc[G] = mfma(A, wr[5 * (RG) + G], acc[G]); }
    WO_STEP(0) WO_STEP(1) WO_STEP(2) WO_STEP(3)
#pragma unroll
    for (int G = 0; G < 5; ++G) {
      auto u = __builtin_amdgcn_permlane32_swap(__float_as_uint(acc[G][0]), __float_as_uint(acc[G][2]), false, false);
      auto v = __builtin_amdgcn_permlane32_swap(__float_as_uint(acc[G][1]), __float_as_uint(acc[G][3]), false, false);
      const f32x2 r = {__uint_as_float(u[0]) + __uint_as_float(u[1]), __uint_as_float(v[0]) + __uint_as_float(v[1])};
      *reinterpret_cast<f32x2*>(&pbuf[w * 640 + 128 * G + 8 * c + 4 * (R & 1) + 2 * (R >> 1)]) = r;
    }
  }
  WG_BARRIER();
  ST(9);
  if (w < 5) {
    f32x2 a = {0.f, 0.f};
#pragma unroll
    for (int q = 0; q < 8; ++q) a += *reinterpret_cast<const f32x2*>(&pbuf[q * 640 + 2 * tid]);
    const u32x4 g = {(__float_as_uint(a[0]) & ~0xffu) | tb_lo, tb_hi, (__float_as_uint(a[1]) & ~0xffu) | tb_lo, tb_hi};
    __builtin_amdgcn_raw_buffer_store_b128(g, rs, WS_FG + ((b * 64 + j) * 640 + 2 * tid) * 8, 0, 16);
  } else if (w == 7 && j < 40) {
    const unsigned foff = WS_FG + ((b * 64 + (lane >> 3)) * 640 + 16 * j + 2 * (lane & 7)) * 8;
    u32x4 fg[8];
    bool ok[8];
#pragma unroll
    for (int k = 0; k < 8; ++k) ok[k] = false;
    for (int spin = 0; spin < SPIN_CAP; ++spin) {
#pragma unroll
      for (int k = 0; k < 8; ++k) if (!ok[k]) fg[k] = __builtin_amdgcn_raw_buffer_load_b128(rs, foff, k * 8 * 640 * 8, 16);
      bool all = true;
#pragma unroll
      for (int k = 0; k < 8; ++k) {
        ok[k] = OK2(fg[k]);
        all &= ok[k];
      }
      if (__all(all)) break;
      (void)RETRY(3);
      __builtin_amdgcn_s_sleep(1);
      asm volatile("" ::: "memory");
    }
    float a0 = 0.f, a1 = 0.f;
#pragma unroll
    for (int k = 0; k < 8; ++k) { a0 += __uint_as_float(fg[k][0] & ~0xffu); a1 += __uint_as_float(fg[k][2] & ~0xffu); }
    a0 += dppf<ROR8>(a0); a1 += dppf<ROR8>(a1);
    a0 = xrow_sum(a0); a1 = xrow_sum(a1);
    if (lane < 8) *reinterpret_cast<unsigned*>(out + b * DM + 16 * j + 2 * lane) = pk_bf16(a0, a1);
    if (j == 0 && lane == 0) {
      const u32x2 e = {(unsigned)tb, (unsigned)(tb >> 32)};
      __builtin_amdgcn_raw_buffer_store_b64(e, rs, WS_E + 8 * b, 0, 16);
    }
  }
}

extern "C" void kernel_launch(void* const* d_in, const int* in_sizes, int n_in, void* d_out, int out_size, void* d_ws,
                              size_t ws_size, hipStream_t stream) {
  fused_kernel<<<256, 512, 0, stream>>>((const u16*)d_in[0], (const u16*)d_in[1], (const u16*)d_in[2], (const u16*)d_in[3],
                                        (const u16*)d_in[4], (const u16*)d_in[6], (const u16*)d_in[7], (const u16*)d_in[8],
                                        (const u16*)d_in[9], (u16*)d_out, (unsigned char*)d_ws);
}
```

```cpp
#include <hip/hip_runtime.h>
#include <hip/hip_bf16.h>
#include <stdint.h>

typedef __attribute__((ext_vector_type(4))) unsigned u32x4;
typedef __attribute__((ext_vector_type(2))) unsigned u32x2;
typedef __attribute__((ext_vector_type(4))) float f32x4;
typedef __attribute__((ext_vector_type(2))) float f32x2;
typedef __attribute__((ext_vector_type(8))) __bf16 bf16x8;
typedef __attribute__((ext_vector_type(2))) __bf16 bf16x2;
typedef unsigned short u16;

constexpr int DM = 640, NH = 32, SK = 4096, WIN = 1024, DH = 128, DF = 1728;
constexpr unsigned WS_E = 0;
constexpr unsigned WS_S1 = 256;
constexpr unsigned WS_ML = WS_S1 + 59u * 4 * 4 * 128 * 8;
constexpr unsigned WS_FG = WS_ML + 4u * 32 * 16 * 2 * 8;
constexpr unsigned WS_END = WS_FG + 4u * 64 * 640 * 8;
constexpr int SPIN_CAP = 1 << 20;

#define SB() __builtin_amdgcn_sched_barrier(0)
#define ST(i) do { } while (0)
#define RETRY(i) 0
#define WG_BARRIER() do { SB(); asm volatile("s_waitcnt lgkmcnt(0)\n\ts_barrier" ::: "memory"); SB(); } while (0)

#define WINDOW 8
#define STR2(x) #x
#define STR(x) STR2(x)
#define PACE() do { SB(); asm volatile("s_waitcnt vmcnt(%0)" :: "n"(WINDOW - 1)); SB(); } while (0)
#define LDK(i) do { PACE(); kr[i] = ldnt(kp + 512 * (i)); } while (0)
#define LDV(i) do { PACE(); vr[i] = ldnt(vp + 512 * (i)); } while (0)
#define LDW(i) do { PACE(); wr[i] = ldnt(wop + ((i) / 5) * 4 * DM + 128 * ((i) % 5)); } while (0)
#define LDD(i) do { PACE(); dr[i] = ldnt(wdp + 128 * (i)); } while (0)
#define POLL(ID, COND, RELOAD) do { if (!__all(COND)) { int spin = 0; do { (void)RETRY(ID); __builtin_amdgcn_s_sleep(1); asm volatile("" ::: "memory"); RELOAD; } \
                                  while (!__all(COND) && ++spin < SPIN_CAP); } } while (0)
#define OK2(G) ((((G)[0] & 0xffu) == tb_lo) & ((G)[1] == tb_hi) & (((G)[2] & 0xffu) == tb_lo) & ((G)[3] == tb_hi))

__device__ __forceinline__ u32x4 ldnt(const void* p) { return __builtin_nontemporal_load(reinterpret_cast<const u32x4*>(p)); }
template <int CTRL> __device__ __forceinline__ unsigned dppu(unsigned x) { return (unsigned)__builtin_amdgcn_mov_dpp((int)x, CTRL, 0xf, 0xf, true); }
template <int CTRL> __device__ __forceinline__ float dppf(float x) { return __uint_as_float(dppu<CTRL>(__float_as_uint(x))); }
constexpr int XOR1 = 0xB1, XOR2 = 0x4E, XOR7 = 0x141, ROR8 = 0x128, ROR4 = 0x124, BCAST = 0x150;
__device__ __forceinline__ float dot2(unsigned a, unsigned b, float c) {
  return __builtin_amdgcn_fdot2_f32_bf16(__builtin_bit_cast(bf16x2, a), __builtin_bit_cast(bf16x2, b), c, false);
}
__device__ __forceinline__ unsigned pk_bf16(float lo, float hi) {
  bf16x2 v = {(__bf16)lo, (__bf16)hi};
  return __builtin_bit_cast(unsigned, v);
}
__device__ __forceinline__ unsigned split_hi_lo(float v) {
  const unsigned h = pk_bf16(v, 0.f) & 0xffffu;
  return pk_bf16(v, v - __uint_as_float(h << 16));
}
__device__ __forceinline__ float xrow_sum(float x) {
  auto s = __builtin_amdgcn_permlane16_swap(__float_as_uint(x), __float_as_uint(x), false, false);
  x = __uint_as_float(s[0]) + __uint_as_float(s[1]);
  auto t = __builtin_amdgcn_permlane32_swap(__float_as_uint(x), __float_as_uint(x), false, false);
  return __uint_as_float(t[0]) + __uint_as_float(t[1]);
}
__device__ __forceinline__ float xrow_max(float x) {
  auto s = __builtin_amdgcn_permlane16_swap(__float_as_uint(x), __float_as_uint(x), false, false);
  x = fmaxf(__uint_as_float(s[0]), __uint_as_float(s[1]));
  auto t = __builtin_amdgcn_permlane32_swap(__float_as_uint(x), __float_as_uint(x), false, false);
  return fmaxf(__uint_as_float(t[0]), __uint_as_float(t[1]));
}
__device__ __forceinline__ float row_sum(float x) { x += dppf<XOR1>(x); x += dppf<XOR2>(x); x += dppf<XOR7>(x); x += dppf<ROR8>(x); return x; }
__device__ __forceinline__ float row_max(float x) { x = fmaxf(x, dppf<XOR1>(x)); x = fmaxf(x, dppf<XOR2>(x)); x = fmaxf(x, dppf<XOR7>(x)); x = fmaxf(x, dppf<ROR8>(x)); return x; }

struct Sel { unsigned s[4]; };
__device__ __forceinline__ bf16x8 diagA(unsigned v, const Sel& sel) {
  u32x4 a = {__builtin_amdgcn_perm(v, v, sel.s[0]), __builtin_amdgcn_perm(v, v, sel.s[1]),
             __builtin_amdgcn_perm(v, v, sel.s[2]), __builtin_amdgcn_perm(v, v, sel.s[3])};
  return __builtin_bit_cast(bf16x8, a);
}
__device__ __forceinline__ f32x4 mfma(bf16x8 a, u32x4 b, f32x4 c) {
  return __builtin_amdgcn_mfma_f32_16x16x32_bf16(a, __builtin_bit_cast(bf16x8, b), c, 0, 0, 0);
}

__global__ __launch_bounds__(512, 2) void fused_kernel(
    const u16* __restrict__ x, const u16* __restrict__ kc, const u16* __restrict__ vc, const u16* __restrict__ wq,
    const u16* __restrict__ wo, const u16* __restrict__ wg, const u16* __restrict__ wu, const u16* __restrict__ wd,
    const u16* __restrict__ gw, u16* __restrict__ out, unsigned char* ws) {
  __shared__ __attribute__((aligned(16))) float s1buf[8 * 512];
  __shared__ __attribute__((aligned(16))) float pbuf[8 * 640];
  __shared__ __attribute__((aligned(16))) float obuf[16 * 132];
  __shared__ __attribute__((aligned(16))) u16 xs[8 * 80];
  __shared__ __attribute__((aligned(16))) unsigned qs[64 + 4];

  ST(0);
  const int blk = blockIdx.x, tq = blk >> 3;
  const int h = (blk & 7) + 8 * (tq >> 3), b = (tq >> 1) & 3, half = tq & 1, j = 2 * h + half;
  const int tid = threadIdx.x, lane = tid & 63, R = lane >> 4, c = lane & 15;
  const int w = __builtin_amdgcn_readfirstlane(tid >> 6);
  const __amdgpu_buffer_rsrc_t rs = __builtin_amdgcn_make_buffer_rsrc(ws, 0, WS_END, 0x00020000);
  const bool has_s1 = (half == 0) || (h < 27);

  const u32x2 ev = __builtin_amdgcn_raw_buffer_load_b64(rs, WS_E + 8 * ((lane & 1) ? (w >> 1) : b), 0, 16);
  const int row0 = 160 * b + 20 * w;
  const int l40 = lane < 40 ? lane : 39;
  const unsigned xv = *reinterpret_cast<const unsigned*>(x + (l40 / 10) * DM + row0 + 2 * (l40 % 10));
  u32x4 gx0, gx1, gg0, gg1;
  if (w == 7) {
    gx0 = *reinterpret_cast<const u32x4*>(x + b * DM + 16 * l40); gx1 = *reinterpret_cast<const u32x4*>(x + b * DM + 16 * l40 + 8);
    gg0 = *reinterpret_cast<const u32x4*>(gw + 16 * l40);          gg1 = *reinterpret_cast<const u32x4*>(gw + 16 * l40 + 8);
  }
  u32x4 s1[5];
  {
    const int ld = half ? DF : NH * DH, hc = h < 27 ? h : h - 8;
    const u16* p = half ? ((c < 8 ? wg : wu) + 64 * hc + 8 * (c & 7)) : (wq + DH * h + 8 * c);
    p += (row0 + R) * ld;
#pragma unroll
    for (int i = 0; i < 5; ++i) s1[i] = ldnt(p + 4 * i * ld);
  }
  SB();
  const size_t kvoff = ((size_t)(b * NH + h) * SK + (SK - WIN) + half * 512 + 64 * w) * DH;
  const u16* kp = kc + kvoff + 8 * lane;
  const u16* vp = vc + kvoff + 8 * lane;
  const u16* wop = wo + (size_t)(h * DH + 16 * w + R) * DM + 8 * c;
  u32x4 kr[16], vr[16], wr[20], dr[5];
  asm volatile("s_barrier");
  kr[0] = ldnt(kp); kr[1] = ldnt(kp + 512); wr[0] = ldnt(wop);
  SB();

  Sel sel;
  {
    const unsigned b0 = 2 * (c >> 3), pr = b0 | ((b0 + 1) << 8), e = c & 7;
    const unsigned on = (e & 1) ? ((pr << 16) | 0x0C0Cu) : (0x0C0C0000u | pr);
#pragma unroll
    for (int q = 0; q < 4; ++q) sel.s[q] = ((e >> 1) == (unsigned)q) ? on : 0x0C0C0C0Cu;
  }
  if (lane < 40) {
    const int bb = lane / 10, rr = 2 * (lane % 10);
    xs[w * 80 + rr * 4 + bb] = (u16)xv; xs[w * 80 + rr * 4 + 4 + bb] = (u16)(xv >> 16);
  }
  if (w == 7) {
    float a = 0.f;
#pragma unroll
    for (int q = 0; q < 4; ++q) { a = dot2(gx0[q], gg0[q], a); a = dot2(gx1[q], gg1[q], a); }
    a = lane < 40 ? a : 0.f;
    a = xrow_sum(row_sum(a));
    if (lane == 0) qs[64] = __float_as_uint(1.f / (1.f + __expf(-a)));
  }
  {
    f32x4 d0 = {0.f, 0.f, 0.f, 0.f}, d1 = d0;
    u32x2 xx[5];
#pragma unroll
    for (int i = 0; i < 5; ++i) xx[i] = *reinterpret_cast<const u32x2*>(&xs[w * 80 + (4 * i + R) * 4]);
#define S1_STEP(i) do { d0 = mfma(diagA(xx[i][0], sel), s1[i], d0); d1 = mfma(diagA(xx[i][1], sel), s1[i], d1); } while (0)
    LDK(2); S1_STEP(0); LDK(3); S1_STEP(1); LDW(1); S1_STEP(2); LDK(4); S1_STEP(3); LDK(5); S1_STEP(4);
    float* dst = &s1buf[w * 512 + (R >> 1) * 128 + 8 * c + 4 * (R & 1)];
    *reinterpret_cast<f32x4*>(dst) = d0; *reinterpret_cast<f32x4*>(dst + 256) = d1;
  }
  ST(1);
  const unsigned long long tb = (((unsigned long long)__builtin_amdgcn_readlane(ev[1], 0) << 32) | (unsigned)__builtin_amdgcn_readlane(ev[0], 0)) + 1;
  const unsigned long long tw = (((unsigned long long)__builtin_amdgcn_readlane(ev[1], 1) << 32) | (unsigned)__builtin_amdgcn_readlane(ev[0], 1)) + 1;
  const unsigned tb_lo = (unsigned)tb & 0xffu, tb_hi = (unsigned)(tb >> 8), tb32 = (unsigned)tb;
  WG_BARRIER();
  if (has_s1 && lane < 32) {
    f32x2 a = {0.f, 0.f};
#pragma unroll
    for (int q = 0; q < 8; ++q) a += *reinterpret_cast<const f32x2*>(&s1buf[q * 512 + w * 64 + 2 * lane]);
    const unsigned lo = (unsigned)tw & 0xffu, hi = (unsigned)(tw >> 8);
    const u32x4 g = {(__float_as_uint(a[0]) & ~0xffu) | lo, hi, (__float_as_uint(a[1]) & ~0xffu) | lo, hi};
    const int tg = half ? 32 + h : h;
    __builtin_amdgcn_raw_buffer_store_b128(g, rs, WS_S1 + (((tg * 4 + (w >> 1)) * 4 + b) * 128 + (w & 1) * 64 + 2 * lane) * 8, 0, 16);
  }
  SB();
  ST(2);
  LDW(2); LDK(6); LDK(7); LDW(3); LDK(8); LDK(9); LDW(4); LDK(10); LDK(11); LDW(5); LDK(12); LDK(13); LDW(6); LDK(14); LDK(15);
  const unsigned qoff = WS_S1 + (((h * 4 + b) * 4 + (lane & 3)) * 128 + 16 * w + 2 * ((lane >> 2) & 7)) * 8;
  u32x4 qg = __builtin_amdgcn_raw_buffer_load_b128(rs, qoff, 0, 16);
  const int fl = 4 * w + R, f = 27 * j + (fl < 27 ? fl : 26);
  const unsigned hoff = WS_S1 + ((((32 + (f >> 6)) * 4 + b) * 4 + (c & 3)) * 128 + ((c >> 2) & 1) * 64 + (f & 63)) * 8;
  const u16* wdp = wd + (size_t)f * DM + 8 * c;
  LDV(0); LDW(7); LDV(1); LDW(8); LDV(2); LDW(9); LDV(3);
  SB();
  POLL(0, OK2(qg), qg = __builtin_amdgcn_raw_buffer_load_b128(rs, qoff, 0, 16));
  ST(3);
  {
    float q0 = __uint_as_float(qg[0] & ~0xffu), q1 = __uint_as_float(qg[2] & ~0xffu);
    q0 += dppf<XOR1>(q0); q1 += dppf<XOR1>(q1);
    q0 += dppf<XOR2>(q0); q1 += dppf<XOR2>(q1);
    if ((lane & 3) == 0 && lane < 32) qs[8 * w + (lane >> 2)] = pk_bf16(q0, q1);
  }
  WG_BARRIER();
  ST(4);
  const u32x4 q4 = *reinterpret_cast<const u32x4*>(&qs[4 * c]);
  const float gate = __uint_as_float(qs[64]);
  float s[16];
#pragma unroll
  for (int i = 0; i < 16; ++i) {
    float a = dot2(kr[i][0], q4[0], 0.f);
    a = dot2(kr[i][1], q4[1], a); a = dot2(kr[i][2], q4[2], a); s[i] = dot2(kr[i][3], q4[3], a);
  }
  {
    const bool b8 = c & 8, b4 = c & 4, b2 = c & 2, b1 = c & 1;
#pragma unroll
    for (int i = 0; i < 8; ++i) s[i] = (b8 ? s[i + 8] : s[i]) + dppf<ROR8>(b8 ? s[i] : s[i + 8]);
#pragma unroll
    for (int i = 0; i < 4; ++i) s[i] = (b4 ? s[i + 4] : s[i]) + dppf<XOR7>(b4 ? s[i] : s[i + 4]);
#pragma unroll
    for (int i = 0; i < 2; ++i) s[i] = (b2 ? s[i + 2] : s[i]) + dppf<XOR2>(b2 ? s[i] : s[i + 2]);
    s[0] = (b1 ? s[1] : s[0]) + dppf<XOR1>(b1 ? s[0] : s[1]);
  }
  const float sc = s[0] * (0.08838834764831845f * 1.4426950408889634f);
  const float mw = xrow_max(row_max(sc));
  const float p = __builtin_amdgcn_exp2f(sc - mw);
  const float lw = xrow_sum(row_sum(p));
  const unsigned mlbase = WS_ML + (b * NH + h) * 256;
  if (lane == 0) {
    const u32x4 g = {__float_as_uint(mw), tb32, __float_as_uint(lw), tb32};
    __builtin_amdgcn_raw_buffer_store_b128(g, rs, mlbase + (half * 8 + w) * 16, 0, 16);
  }
  const unsigned pp = split_hi_lo(p);
  SB();
  ST(5);
  u32x2 hg = __builtin_amdgcn_raw_buffer_load_b64(rs, hoff, 0, 16);
  f32x4 o0 = {0.f, 0.f, 0.f, 0.f}, o1 = o0;
#define PV_STEP(I, ACC) ACC = mfma(diagA(dppu<BCAST + (I)>(pp), sel), vr[I], ACC)
  LDV(4); LDD(0); LDV(5); LDD(1); PV_STEP(0, o0); LDV(6); LDD(2); PV_STEP(1, o1); LDV(7); LDD(3); PV_STEP(2, o0);
  LDV(8); LDD(4); PV_STEP(3, o1); LDW(10); LDV(9); PV_STEP(4, o0); LDW(11); LDW(12); PV_STEP(5, o1); LDV(10); LDW(13); PV_STEP(6, o0);
  LDW(14); LDV(11); PV_STEP(7, o1);
  ST(6);
  f32x4 acc[5];
#pragma unroll
  for (int G = 0; G < 5; ++G) acc[G] = f32x4{0.f, 0.f, 0.f, 0.f};
  {
    POLL(1, ((hg[0] & 0xffu) == tb_lo) & (hg[1] == tb_hi), hg = __builtin_amdgcn_raw_buffer_load_b64(rs, hoff, 0, 16));
    float a = __uint_as_float(hg[0] & ~0xffu);
    a += dppf<XOR1>(a); a += dppf<XOR2>(a);
    const float gv = dppf<BCAST + 0>(a), uv = dppf<BCAST + 4>(a);
    const float z2 = 1.5957691216057308f * (gv + 0.044715f * gv * gv * gv);
    float hv = gv / (1.f + __expf(-z2)) * uv * (1.f - gate);
    hv = fl < 27 ? hv : 0.f;
    const bf16x8 A = diagA(split_hi_lo(hv), sel);
#pragma unroll
    for (int G = 0; G < 5; ++G) acc[G] = mfma(A, dr[G], acc[G]);
  }
  ST(7);
  u32x4 mg = __builtin_amdgcn_raw_buffer_load_b128(rs, mlbase + c * 16, 0, 16);
  LDW(15); LDV(12); PV_STEP(8, o0); LDW(16); LDV(13); PV_STEP(9, o1); LDW(17); LDV(14); LDW(18); PV_STEP(10, o0); LDW(19); LDV(15);
  PV_STEP(11, o1);
  SB();
  POLL(2, (mg[1] == tb32) & (mg[3] == tb32), mg = __builtin_amdgcn_raw_buffer_load_b128(rs, mlbase + c * 16, 0, 16));
  {
    const float mi = __uint_as_float(mg[0]), li = __uint_as_float(mg[2]);
    const float M = row_max(mi);
    const float L = row_sum(li * __builtin_amdgcn_exp2f(mi - M));
    const float scale = __builtin_amdgcn_exp2f(mw - M) / L * gate;
    SB();
    PV_STEP(12, o0); PV_STEP(13, o1); PV_STEP(14, o0); PV_STEP(15, o1);
    o0 = (o0 + o1) * scale;
    *reinterpret_cast<f32x4*>(&obuf[(2 * w + (R >> 1)) * 132 + 8 * c + 4 * (R & 1)]) = o0;
  }
  WG_BARRIER();
  ST(8);
  {
    const float* op = &obuf[(c >> 2) * 4 * 132 + 16 * w + 4 * (c & 3) + R];
    float o = (op[0] + op[132]) + (op[264] + op[396]);
    o += dppf<ROR4>(o); o += dppf<ROR8>(o);
    const unsigned ov = split_hi_lo(o);
#define WO_STEP(RG) { const bf16x8 A = diagA(dppu<(RG) * 0x55>(ov), sel); \
    _Pragma("unroll") for (int G = 0; G < 5; ++G) acc[G] = mfma(A, wr[5 * (RG) + G], acc[G]); }
    WO_STEP(0) WO_STEP(1) WO_STEP(2) WO_STEP(3)
#pragma unroll
    for (int G = 0; G < 5; ++G) {
      auto u = __builtin_amdgcn_permlane32_swap(__float_as_uint(acc[G][0]), __float_as_uint(acc[G][2]), false, false);
      auto v = __builtin_amdgcn_permlane32_swap(__float_as_uint(acc[G][1]), __float_as_uint(acc[G][3]), false, false);
      const f32x2 r = {__uint_as_float(u[0]) + __uint_as_float(u[1]), __uint_as_float(v[0]) + __uint_as_float(v[1])};
      *reinterpret_cast<f32x2*>(&pbuf[w * 640 + 128 * G + 8 * c + 4 * (R & 1) + 2 * (R >> 1)]) = r;
    }
  }
  WG_BARRIER();
  ST(9);
  if (w < 5) {
    f32x2 a = {0.f, 0.f};
#pragma unroll
    for (int q = 0; q < 8; ++q) a += *reinterpret_cast<const f32x2*>(&pbuf[q * 640 + 2 * tid]);
    const u32x4 g = {(__float_as_uint(a[0]) & ~0xffu) | tb_lo, tb_hi, (__float_as_uint(a[1]) & ~0xffu) | tb_lo, tb_hi};
    __builtin_amdgcn_raw_buffer_store_b128(g, rs, WS_FG + ((b * 64 + j) * 640 + 2 * tid) * 8, 0, 16);
  } else if (w == 7 && j < 40) {
    const unsigned foff = WS_FG + ((b * 64 + (lane >> 3)) * 640 + 16 * j + 2 * (lane & 7)) * 8;
    u32x4 fg[8];
    bool ok[8];
#pragma unroll
    for (int k = 0; k < 8; ++k) ok[k] = false;
    for (int spin = 0; spin < SPIN_CAP; ++spin) {
#pragma unroll
      for (int k = 0; k < 8; ++k) if (!ok[k]) fg[k] = __builtin_amdgcn_raw_buffer_load_b128(rs, foff, k * 8 * 640 * 8, 16);
      bool all = true;
#pragma unroll
      for (int k = 0; k < 8; ++k) {
        ok[k] = OK2(fg[k]);
        all &= ok[k];
      }
      if (__all(all)) break;
      (void)RETRY(3);
      __builtin_amdgcn_s_sleep(1);
      asm volatile("" ::: "memory");
    }
    float a0 = 0.f, a1 = 0.f;
#pragma unroll
    for (int k = 0; k < 8; ++k) { a0 += __uint_as_float(fg[k][0] & ~0xffu); a1 += __uint_as_float(fg[k][2] & ~0xffu); }
    a0 += dppf<ROR8>(a0); a1 += dppf<ROR8>(a1);
    a0 = xrow_sum(a0); a1 = xrow_sum(a1);
    if (lane < 8) *reinterpret_cast<unsigned*>(out + b * DM + 16 * j + 2 * lane) = pk_bf16(a0, a1);
    if (j == 0 && lane == 0) {
      const u32x2 e = {(unsigned)tb, (unsigned)(tb >> 32)};
      __builtin_amdgcn_raw_buffer_store_b64(e, rs, WS_E + 8 * b, 0, 16);
    }
  }
}

extern "C" void kernel_launch(void* const* d_in, const int* in_sizes, int n_in, void* d_out, int out_size, void* d_ws,
                              size_t ws_size, hipStream_t stream) {
  fused_kernel<<<256, 512, 0, stream>>>((const u16*)d_in[0], (const u16*)d_in[1], (const u16*)d_in[2], (const u16*)d_in[3],
                                        (const u16*)d_in[4], (const u16*)d_in[6], (const u16*)d_in[7], (const u16*)d_in[8],
                                        (const u16*)d_in[9], (u16*)d_out, (unsigned char*)d_ws);
}
```

```cpp
#include <hip/hip_runtime.h>
#include <hip/hip_bf16.h>
#include <stdint.h>

typedef __attribute__((ext_vector_type(4))) unsigned u32x4;
typedef __attribute__((ext_vector_type(2))) unsigned u32x2;
typedef __attribute__((ext_vector_type(4))) float f32x4;
typedef __attribute__((ext_vector_type(2))) float f32x2;
typedef __attribute__((ext_vector_type(8))) __bf16 bf16x8;
typedef __attribute__((ext_vector_type(2))) __bf16 bf16x2;
typedef unsigned short u16;

constexpr int DM = 640, NH = 32, SK = 4096, WIN = 1024, DH = 128, DF = 1728;
constexpr unsigned WS_E = 0;
constexpr unsigned WS_S1 = 256;
constexpr unsigned WS_ML = WS_S1 + 59u * 4 * 4 * 128 * 8;
constexpr unsigned WS_FG = WS_ML + 4u * 32 * 16 * 2 * 8;
constexpr unsigned WS_END = WS_FG + 4u * 64 * 640 * 8;
constexpr int SPIN_CAP = 1 << 20;

#define SB() __builtin_amdgcn_sched_barrier(0)
#define ST(i) do { } while (0)
#define RETRY(i) 0
#define WG_BARRIER() do { SB(); asm volatile("s_waitcnt lgkmcnt(0)\n\ts_barrier" ::: "memory"); SB(); } while (0)

#define LDK(i) do { kr[i] = ldnt(kp + 512 * (i)); SB(); } while (0)
#define LDV(i) do { vr[i] = ldnt(vp + 512 * (i)); SB(); } while (0)
#define WO_AUX 2
#define LDW(i) do { wr[i] = __builtin_amdgcn_raw_buffer_load_b128(rwo, wooff + 256 * ((i) % 5), ((i) / 5) * 4 * DM * 2, WO_AUX); SB(); } while (0)
#define LDS_WAIT(PTR, VAL) do { int spin = 0; while (__hip_atomic_load(PTR, __ATOMIC_RELAXED, __HIP_MEMORY_SCOPE_WORKGROUP) < (VAL) && ++spin < SPIN_CAP) \
                                  __builtin_amdgcn_s_sleep(0); asm volatile("" ::: "memory"); SB(); } while (0)
#define LDS_DRAIN() do { SB(); asm volatile("s_waitcnt lgkmcnt(0)" ::: "memory"); SB(); } while (0)
#define POLL(ID, COND, RELOAD) do { if (!__all(COND)) { int spin = 0; do { (void)RETRY(ID); __builtin_amdgcn_s_sleep(1); asm volatile("" ::: "memory"); RELOAD; } \
                                  while (!__all(COND) && ++spin < SPIN_CAP); } } while (0)
#define OK2(G) ((((G)[0] & 0xffu) == tb_lo) & ((G)[1] == tb_hi) & (((G)[2] & 0xffu) == tb_lo) & ((G)[3] == tb_hi))

__device__ __forceinline__ u32x4 ldnt(const void* p) { return __builtin_nontemporal_load(reinterpret_cast<const u32x4*>(p)); }
template <int CTRL> __device__ __forceinline__ unsigned dppu(unsigned x) { return (unsigned)__builtin_amdgcn_mov_dpp((int)x, CTRL, 0xf, 0xf, true); }
template <int CTRL> __device__ __forceinline__ float dppf(float x) { return __uint_as_float(dppu<CTRL>(__float_as_uint(x))); }
constexpr int XOR1 = 0xB1, XOR2 = 0x4E, XOR7 = 0x141, ROR8 = 0x128, ROR4 = 0x124, BCAST = 0x150;
__device__ __forceinline__ float dot2(unsigned a, unsigned b, float c) {
  return __builtin_amdgcn_fdot2_f32_bf16(__builtin_bit_cast(bf16x2, a), __builtin_bit_cast(bf16x2, b), c, false);
}
__device__ __forceinline__ unsigned pk_bf16(float lo, float hi) {
  bf16x2 v = {(__bf16)lo, (__bf16)hi};
  return __builtin_bit_cast(unsigned, v);
}
__device__ __forceinline__ unsigned split_hi_lo(float v) {
  const unsigned h = pk_bf16(v, 0.f) & 0xffffu;
  return pk_bf16(v, v - __uint_as_float(h << 16));
}
__device__ __forceinline__ float xrow_sum(float x) {
  auto s = __builtin_amdgcn_permlane16_swap(__float_as_uint(x), __float_as_uint(x), false, false);
  x = __uint_as_float(s[0]) + __uint_as_float(s[1]);
  auto t = __builtin_amdgcn_permlane32_swap(__float_as_uint(x), __float_as_uint(x), false, false);
  return __uint_as_float(t[0]) + __uint_as_float(t[1]);
}
__device__ __forceinline__ float xrow_max(float x) {
  auto s = __builtin_amdgcn_permlane16_swap(__float_as_uint(x), __float_as_uint(x), false, false);
  x = fmaxf(__uint_as_float(s[0]), __uint_as_float(s[1]));
  auto t = __builtin_amdgcn_permlane32_swap(__float_as_uint(x), __float_as_uint(x), false, false);
  return fmaxf(__uint_as_float(t[0]), __uint_as_float(t[1]));
}
__device__ __forceinline__ float row_sum(float x) { x += dppf<XOR1>(x); x += dppf<XOR2>(x); x += dppf<XOR7>(x); x += dppf<ROR8>(x); return x; }
__device__ __forceinline__ float row_max(float x) { x = fmaxf(x, dppf<XOR1>(x)); x = fmaxf(x, dppf<XOR2>(x)); x = fmaxf(x, dppf<XOR7>(x)); x = fmaxf(x, dppf<ROR8>(x)); return x; }

struct Sel { unsigned s[4]; };
__device__ __forceinline__ bf16x8 diagA(unsigned v, const Sel& sel) {
  u32x4 a = {__builtin_amdgcn_perm(v, v, sel.s[0]), __builtin_amdgcn_perm(v, v, sel.s[1]),
             __builtin_amdgcn_perm(v, v, sel.s[2]), __builtin_amdgcn_perm(v, v, sel.s[3])};
  return __builtin_bit_cast(bf16x8, a);
}
__device__ __forceinline__ f32x4 mfma(bf16x8 a, u32x4 b, f32x4 c) {
  return __builtin_amdgcn_mfma_f32_16x16x32_bf16(a, __builtin_bit_cast(bf16x8, b), c, 0, 0, 0);
}

__global__ __launch_bounds__(512, 2) void fused_kernel(
    const u16* __restrict__ x, const u16* __restrict__ kc, const u16* __restrict__ vc, const u16* __restrict__ wq,
    const u16* __restrict__ wo, const u16* __restrict__ wg, const u16* __restrict__ wu, const u16* __restrict__ wd,
    const u16* __restrict__ gw, u16* __restrict__ out, unsigned char* ws) {
  __shared__ __attribute__((aligned(16))) float s1buf[4 * 512];
  __shared__ __attribute__((aligned(16))) float pbuf[8 * 640];
  __shared__ __attribute__((aligned(16))) float obuf[8 * 132];
  __shared__ __attribute__((aligned(16))) u16 xs[4 * 160];
  __shared__ __attribute__((aligned(16))) unsigned ps[4 * 128];
  __shared__ __attribute__((aligned(16))) unsigned flg[16];

  ST(0);
  const int blk = blockIdx.x, tq = blk >> 3;

  const int h = 4 * (blk & 7) + (tq >> 3),
             b = (tq >> 1) & 3, half = tq & 1, j = 2 * h + half;
  const int tid = threadIdx.x, lane = tid & 63, R = lane >> 4, c = lane & 15;
  const int w = __builtin_amdgcn_readfirstlane(tid >> 6), w4 = w & 3;
  const __amdgpu_buffer_rsrc_t rs = __builtin_amdgcn_make_buffer_rsrc(ws, 0, WS_END, 0x00020000);
  const size_t kvoff = ((size_t)(b * NH + h) * SK + (SK - WIN) + half * 512 + 128 * w4) * DH;
  const __amdgpu_buffer_rsrc_t rwo = __builtin_amdgcn_make_buffer_rsrc((void*)(wo + (size_t)(h * DH + 16 * w) * DM), 0, 16 * DM * 2, 0x00020000);
  const unsigned wooff = (R * DM + 8 * c) * 2;
  const u32x2 ev = __builtin_amdgcn_raw_buffer_load_b64(rs, WS_E + 8 * ((lane & 1) ? w4 : b), 0, 16);
  u32x4 wr[20];
  f32x4 acc[5];
#pragma unroll
  for (int G = 0; G < 5; ++G) acc[G] = f32x4{0.f, 0.f, 0.f, 0.f};
  Sel sel;
  unsigned long long tb;
  unsigned tb_lo, tb_hi;
#define MAKE_SEL() do { const unsigned b0 = 2 * (c >> 3), pr = b0 | ((b0 + 1) << 8), e = c & 7; \
    const unsigned on = (e & 1) ? ((pr << 16) | 0x0C0Cu) : (0x0C0C0000u | pr); \
    _Pragma("unroll") for (int q = 0; q < 4; ++q) sel.s[q] = ((e >> 1) == (unsigned)q) ? on : 0x0C0C0C0Cu; } while (0)
#define MAKE_TAG() do { tb = (((unsigned long long)__builtin_amdgcn_readlane(ev[1], 0) << 32) | (unsigned)__builtin_amdgcn_readlane(ev[0], 0)) + 1; \
    tb_lo = (unsigned)tb & 0xffu; tb_hi = (unsigned)(tb >> 8); } while (0)

  if (w < 4) {
    __builtin_amdgcn_s_setprio(2);
    const bool has_s1 = (half == 0) || (h < 27);
    const int row0 = 160 * b + 40 * w;
    const int l40 = lane < 40 ? lane : 39;
    const u32x2 xv = *reinterpret_cast<const u32x2*>(x + (l40 / 10) * DM + row0 + 4 * (l40 % 10));
    u32x4 s1[10], kr[32];
    {
      const int ld = half ? DF : NH * DH, hc = h < 27 ? h : 26;
      const u16* p = half ? ((c < 8 ? wg : wu) + 64 * hc + 8 * (c & 7)) : (wq + DH * h + 8 * c);
      p += (row0 + R) * ld;
#pragma unroll
      for (int i = 0; i < 10; ++i) s1[i] = ldnt(p + 4 * i * ld);
    }
    const u32x4 gx0 = *reinterpret_cast<const u32x4*>(x + b * DM + 16 * l40), gx1 = *reinterpret_cast<const u32x4*>(x + b * DM + 16 * l40 + 8);
    const u32x4 gg0 = *reinterpret_cast<const u32x4*>(gw + 16 * l40), gg1 = *reinterpret_cast<const u32x4*>(gw + 16 * l40 + 8);
    if (tid < 16) flg[tid] = 0;
    WG_BARRIER();
    const u16* kp = kc + kvoff + 8 * lane;
    LDK(0); LDK(1); LDK(2); LDK(3);
    MAKE_SEL();
    if (lane < 40) {
      u16* d = &xs[w * 160 + (lane % 10) * 16 + lane / 10];
      d[0] = (u16)xv[0]; d[4] = (u16)(xv[0] >> 16); d[8] = (u16)xv[1]; d[12] = (u16)(xv[1] >> 16);
    }
    {
      f32x4 d0 = {0.f, 0.f, 0.f, 0.f}, d1 = d0;
#pragma unroll
      for (int i = 0; i < 10; ++i) {
        const u32x2 xx = *reinterpret_cast<const u32x2*>(&xs[w * 160 + (4 * i + R) * 4]);
        d0 = mfma(diagA(xx[0], sel), s1[i], d0); d1 = mfma(diagA(xx[1], sel), s1[i], d1);
      }
      float* dst = &s1buf[w * 512 + (R >> 1) * 128 + 8 * c + 4 * (R & 1)];
      *reinterpret_cast<f32x4*>(dst) = d0; *reinterpret_cast<f32x4*>(dst + 256) = d1;
    }
    LDS_DRAIN();
    if (lane == 0) __hip_atomic_fetch_add(&flg[0], 1u, __ATOMIC_RELAXED, __HIP_MEMORY_SCOPE_WORKGROUP);
    ST(1);
    MAKE_TAG();
    const unsigned long long tw = (((unsigned long long)__builtin_amdgcn_readlane(ev[1], 1) << 32) | (unsigned)__builtin_amdgcn_readlane(ev[0], 1)) + 1;
    float gate;
    {
      float a = 0.f;
#pragma unroll
      for (int q = 0; q < 4; ++q) { a = dot2(gx0[q], gg0[q], a); a = dot2(gx1[q], gg1[q], a); }
      a = lane < 40 ? a : 0.f;
      a = xrow_sum(row_sum(a));
      gate = 1.f / (1.f + __expf(-a));
    }
    LDS_WAIT(&flg[0], 4u);
    if (has_s1) {
      f32x2 a = {0.f, 0.f};
#pragma unroll
      for (int q = 0; q < 4; ++q) a += *reinterpret_cast<const f32x2*>(&s1buf[q * 512 + w * 128 + 2 * lane]);
      const unsigned lo = (unsigned)tw & 0xffu, hi = (unsigned)(tw >> 8);
      const u32x4 g = {(__float_as_uint(a[0]) & ~0xffu) | lo, hi, (__float_as_uint(a[1]) & ~0xffu) | lo, hi};
      const int tg = half ? 32 + h : h;
      __builtin_amdgcn_raw_buffer_store_b128(g, rs, WS_S1 + (tg * 4 + w) * 4096 + ((lane & 3) * 64 + b * 16 + (lane >> 2)) * 16, 0, 16);
    }
    SB();
    ST(2);
#pragma unroll
    for (int i = 0; i < 6; ++i) { LDK(4 + 3 * i); LDK(5 + 3 * i); LDK(6 + 3 * i); LDW(i); }
    const unsigned qoff = WS_S1 + (h * 4 + b) * 4096 + lane * 16;
    u32x4 qg[4];
#define LDQ() do { _Pragma("unroll") for (int q = 0; q < 4; ++q) qg[q] = __builtin_amdgcn_raw_buffer_load_b128(rs, qoff + 1024 * q, 0, 16); } while (0)
    LDQ();
    SB();
    LDK(22); LDK(23); LDK(24); LDK(25); LDK(26); LDW(6); LDK(27); LDK(28); LDK(29); LDK(30); LDK(31); LDW(7);
    POLL(0, OK2(qg[0]) & OK2(qg[1]) & OK2(qg[2]) & OK2(qg[3]), LDQ());
    ST(3);
    u32x4 q4;
#pragma unroll
    for (int q = 0; q < 4; ++q) q4[q] = pk_bf16(xrow_sum(__uint_as_float(qg[q][0] & ~0xffu)), xrow_sum(__uint_as_float(qg[q][2] & ~0xffu)));
    ST(4);
    float sc[2];
    const bool b8 = c & 8, b4 = c & 4, b2 = c & 2, b1 = c & 1;
#pragma unroll
    for (int t = 0; t < 2; ++t) {
      float s[16];
#pragma unroll
      for (int i = 0; i < 16; ++i) {
        float a = dot2(kr[16 * t + i][0], q4[0], 0.f);
        a = dot2(kr[16 * t + i][1], q4[1], a); a = dot2(kr[16 * t + i][2], q4[2], a); s[i] = dot2(kr[16 * t + i][3], q4[3], a);
      }
#pragma unroll
      for (int i = 0; i < 8; ++i) s[i] = (b8 ? s[i + 8] : s[i]) + dppf<ROR8>(b8 ? s[i] : s[i + 8]);
#pragma unroll
      for (int i = 0; i < 4; ++i) s[i] = (b4 ? s[i + 4] : s[i]) + dppf<XOR7>(b4 ? s[i] : s[i + 4]);
#pragma unroll
      for (int i = 0; i < 2; ++i) s[i] = (b2 ? s[i + 2] : s[i]) + dppf<XOR2>(b2 ? s[i] : s[i + 2]);
      sc[t] = ((b1 ? s[1] : s[0]) + dppf<XOR1>(b1 ? s[0] : s[1])) * (0.08838834764831845f * 1.4426950408889634f);
      SB();
    }
    const float mw = xrow_max(row_max(fmaxf(sc[0], sc[1])));
    const float p0 = __builtin_amdgcn_exp2f(sc[0] - mw), p1 = __builtin_amdgcn_exp2f(sc[1] - mw);
    *reinterpret_cast<u32x2*>(&ps[w * 128 + 2 * lane]) = u32x2{split_hi_lo(p0), split_hi_lo(p1)};
    LDS_DRAIN();
    if (lane == 0) __hip_atomic_store(&flg[4 + w], 1u, __ATOMIC_RELAXED, __HIP_MEMORY_SCOPE_WORKGROUP);
    const float lw = xrow_sum(row_sum(p0 + p1));
    const unsigned mlbase = WS_ML + (b * NH + h) * 256, tb32 = (unsigned)tb;
    if (lane == 0) {
      const u32x4 g = {__float_as_uint(mw), tb32, __float_as_uint(lw), tb32};
      __builtin_amdgcn_raw_buffer_store_b128(g, rs, mlbase + (half * 4 + w) * 16, 0, 16);
    }
    SB();
    ST(5);
    const int fl = 8 * w + 4 * (c >> 3) + R, f = 27 * j + (fl < 27 ? fl : 26);
    const int hcol = ((c >> 2) & 1) * 64 + (f & 63);
    const unsigned hoff = WS_S1 + ((32 + (f >> 6)) * 4 + b) * 4096 + (((hcol >> 1) & 3) * 64 + (c & 3) * 16 + (hcol >> 3)) * 16 + (hcol & 1) * 8;
    u32x2 hg = __builtin_amdgcn_raw_buffer_load_b64(rs, hoff, 0, 16);
    u32x4 dr[10];
    {
      const int f0 = 8 * w + R, f1 = f0 + 4;
      const __amdgpu_buffer_rsrc_t rwd = __builtin_amdgcn_make_buffer_rsrc((void*)(wd + (size_t)27 * j * DM), 0, 27 * DM * 2, 0x00020000);
      const unsigned d0 = ((f0 < 27 ? f0 : 26) * DM + 8 * c) * 2, d1 = ((f1 < 27 ? f1 : 26) * DM + 8 * c) * 2;
#pragma unroll
      for (int G = 0; G < 5; ++G) dr[G] = __builtin_amdgcn_raw_buffer_load_b128(rwd, d0 + 256 * G, 0, WO_AUX);
#pragma unroll
      for (int G = 0; G < 5; ++G) dr[5 + G] = __builtin_amdgcn_raw_buffer_load_b128(rwd, d1 + 256 * G, 0, WO_AUX);
    }
    SB();
    LDW(8); LDW(9); LDW(10); LDW(11); LDW(12); LDW(13);
    u32x4 mg = __builtin_amdgcn_raw_buffer_load_b128(rs, mlbase + (c & 7) * 16, 0, 16);
    SB();
    LDW(14); LDW(15); LDW(16); LDW(17); LDW(18); LDW(19);
    ST(6);
    {
      POLL(1, ((hg[0] & 0xffu) == tb_lo) & (hg[1] == tb_hi), hg = __builtin_amdgcn_raw_buffer_load_b64(rs, hoff, 0, 16));
      float a = __uint_as_float(hg[0] & ~0xffu);
      a += dppf<XOR1>(a); a += dppf<XOR2>(a);
      const float other = dppf<XOR7>(a);
      const float gv = (c & 4) ? other : a, uv = (c & 4) ? a : other;
      const float z2 = 1.5957691216057308f * (gv + 0.044715f * gv * gv * gv);
      float hv = gv / (1.f + __expf(-z2)) * uv * (1.f - gate);
      hv = fl < 27 ? hv : 0.f;
      const unsigned hh = split_hi_lo(hv);
      const bf16x8 A0 = diagA(dppu<BCAST + 0>(hh), sel), A1 = diagA(dppu<BCAST + 8>(hh), sel);
#pragma unroll
      for (int G = 0; G < 5; ++G) acc[G] = mfma(A0, dr[G], acc[G]);
#pragma unroll
      for (int G = 0; G < 5; ++G) acc[G] = mfma(A1, dr[5 + G], acc[G]);
    }
    SB();
    POLL(2, (mg[1] == tb32) & (mg[3] == tb32), mg = __builtin_amdgcn_raw_buffer_load_b128(rs, mlbase + (c & 7) * 16, 0, 16));
    {
      const float mi = __uint_as_float(mg[0]), li = __uint_as_float(mg[2]);
      float M = fmaxf(mi, dppf<XOR1>(mi)); M = fmaxf(M, dppf<XOR2>(M)); M = fmaxf(M, dppf<XOR7>(M));
      float L = li * __builtin_amdgcn_exp2f(mi - M);
      L += dppf<XOR1>(L); L += dppf<XOR2>(L); L += dppf<XOR7>(L);
      if (lane == 0) flg[8 + w] = __float_as_uint(__builtin_amdgcn_exp2f(mw - M) / L * gate);
    }
    ST(7);
  } else {
    WG_BARRIER();
    const u16* vp = vc + kvoff + 8 * lane;
    u32x4 vr[32];
#pragma unroll
    for (int i = 0; i < 16; ++i) { LDV(2 * i); LDV(2 * i + 1); LDW(i); }
    LDW(16); LDW(17); LDW(18); LDW(19);
    ST(5);
    MAKE_SEL();
    MAKE_TAG();
    LDS_WAIT(&flg[w], 1u);
    ST(6);
    const u32x2 pp = *reinterpret_cast<const u32x2*>(&ps[w4 * 128 + 2 * lane]);
    f32x4 o0 = {0.f, 0.f, 0.f, 0.f}, o1 = o0;
#define PV2(I) do { o0 = mfma(diagA(dppu<BCAST + ((I) & 15)>(pp[(I) >> 4]), sel), vr[I], o0); \
                    o1 = mfma(diagA(dppu<BCAST + (((I) + 1) & 15)>(pp[(I) >> 4]), sel), vr[(I) + 1], o1); SB(); } while (0)
    PV2(0); PV2(2); PV2(4); PV2(6); PV2(8); PV2(10); PV2(12); PV2(14);
    PV2(16); PV2(18); PV2(20); PV2(22); PV2(24); PV2(26); PV2(28); PV2(30);
    *reinterpret_cast<f32x4*>(&obuf[(2 * w4 + (R >> 1)) * 132 + 8 * c + 4 * (R & 1)]) = o0 + o1;
    ST(7);
  }
  WG_BARRIER();
  ST(8);
  {
    const float* op = &obuf[(c >> 2) * 2 * 132 + 16 * w + 4 * (c & 3) + R];
    float o = (op[0] + op[132]) * __uint_as_float(flg[8 + (c >> 2)]);
    o += dppf<ROR4>(o); o += dppf<ROR8>(o);
    const unsigned ov = split_hi_lo(o);
#define WO_STEP(RG) { const bf16x8 A = diagA(dppu<(RG) * 0x55>(ov), sel); \
    _Pragma("unroll") for (int G = 0; G < 5; ++G) acc[G] = mfma(A, wr[5 * (RG) + G], acc[G]); }
    WO_STEP(0) WO_STEP(1) WO_STEP(2) WO_STEP(3)
#pragma unroll
    for (int G = 0; G < 5; ++G) {
      auto u = __builtin_amdgcn_permlane32_swap(__float_as_uint(acc[G][0]), __float_as_uint(acc[G][2]), false, false);
      auto v = __builtin_amdgcn_permlane32_swap(__float_as_uint(acc[G][1]), __float_as_uint(acc[G][3]), false, false);
      const f32x2 r = {__uint_as_float(u[0]) + __uint_as_float(u[1]), __uint_as_float(v[0]) + __uint_as_float(v[1])};
      *reinterpret_cast<f32x2*>(&pbuf[w * 640 + 128 * G + 8 * c + 4 * (R & 1) + 2 * (R >> 1)]) = r;
    }
  }
  WG_BARRIER();
  ST(9);
  if (w < 5) {
    f32x2 a = {0.f, 0.f};
#pragma unroll
    for (int q = 0; q < 8; ++q) a += *reinterpret_cast<const f32x2*>(&pbuf[q * 640 + 2 * tid]);
    const u32x4 g = {(__float_as_uint(a[0]) & ~0xffu) | tb_lo, tb_hi, (__float_as_uint(a[1]) & ~0xffu) | tb_lo, tb_hi};
    __builtin_amdgcn_raw_buffer_store_b128(g, rs, WS_FG + ((b * 64 + j) * 640 + 2 * tid) * 8, 0, 16);
  } else if (w == 7 && j < 40) {
    const unsigned foff = WS_FG + ((b * 64 + (lane >> 3)) * 640 + 16 * j + 2 * (lane & 7)) * 8;
    u32x4 fg[8];
    bool ok[8];
#pragma unroll
    for (int k = 0; k < 8; ++k) ok[k] = false;
    for (int spin = 0; spin < SPIN_CAP; ++spin) {
#pragma unroll
      for (int k = 0; k < 8; ++k) if (!ok[k]) fg[k] = __builtin_amdgcn_raw_buffer_load_b128(rs, foff, k * 8 * 640 * 8, 16);
      bool all = true;
#pragma unroll
      for (int k = 0; k < 8; ++k) {
        ok[k] = OK2(fg[k]);
        all &= ok[k];
      }
      if (__all(all)) break;
      (void)RETRY(3);
      __builtin_amdgcn_s_sleep(1);
      asm volatile("" ::: "memory");
    }
    float a0 = 0.f, a1 = 0.f;
#pragma unroll
    for (int k = 0; k < 8; ++k) { a0 += __uint_as_float(fg[k][0] & ~0xffu); a1 += __uint_as_float(fg[k][2] & ~0xffu); }
    a0 += dppf<ROR8>(a0); a1 += dppf<ROR8>(a1);
    a0 = xrow_sum(a0); a1 = xrow_sum(a1);
    if (lane < 8) *reinterpret_cast<unsigned*>(out + b * DM + 16 * j + 2 * lane) = pk_bf16(a0, a1);
    if (j == 0 && lane == 0) {
      const u32x2 e = {(unsigned)tb, (unsigned)(tb >> 32)};
      __builtin_amdgcn_raw_buffer_store_b64(e, rs, WS_E + 8 * b, 0, 16);
    }
  }
}

extern "C" void kernel_launch(void* const* d_in, const int* in_sizes, int n_in, void* d_out, int out_size, void* d_ws,
                              size_t ws_size, hipStream_t stream) {
  fused_kernel<<<256, 512, 0, stream>>>((const u16*)d_in[0], (const u16*)d_in[1], (const u16*)d_in[2], (const u16*)d_in[3],
                                        (const u16*)d_in[4], (const u16*)d_in[6], (const u16*)d_in[7], (const u16*)d_in[8],
                                        (const u16*)d_in[9], (u16*)d_out, (unsigned char*)d_ws);
}
```

```cpp
#include <hip/hip_runtime.h>
#include <hip/hip_bf16.h>
#include <stdint.h>

typedef __attribute__((ext_vector_type(4))) unsigned u32x4;
typedef __attribute__((ext_vector_type(2))) unsigned u32x2;
typedef __attribute__((ext_vector_type(4))) float f32x4;
typedef __attribute__((ext_vector_type(2))) float f32x2;
typedef __attribute__((ext_vector_type(8))) __bf16 bf16x8;
typedef __attribute__((ext_vector_type(2))) __bf16 bf16x2;
typedef unsigned short u16;

constexpr int DM = 640, NH = 32, SK = 4096, WIN = 1024, DH = 128, DF = 1728;
constexpr unsigned WS_E = 0;
constexpr unsigned WS_S1 = 256;
constexpr unsigned WS_ML = WS_S1 + 59u * 4 * 4 * 128 * 8;
constexpr unsigned WS_FG = WS_ML + 4u * 32 * 16 * 2 * 8;
constexpr unsigned WS_END = WS_FG + 4u * 64 * 640 * 8;
constexpr unsigned CODE_BYTES = 11800;
constexpr int SPIN_CAP = 1 << 20;
constexpr unsigned OOB = 0x80000000u;

#define SB() __builtin_amdgcn_sched_barrier(0)
#define WG_BARRIER() do { SB(); asm volatile("s_waitcnt lgkmcnt(0)\n\ts_barrier" ::: "memory"); SB(); } while (0)

#define KV_STRIDE 1024
#define KV_WOFF 32768
#define LDK(i) do { kr[i] = __builtin_amdgcn_raw_buffer_load_b128(rkv, lane * 16, (i) * KV_STRIDE, 2); SB(); } while (0)
#define LDV(i) do { vr[i] = __builtin_amdgcn_raw_buffer_load_b128(rkv, lane * 16, (i) * KV_STRIDE, 2); SB(); } while (0)
#define LDW(i) do { wr[i] = __builtin_amdgcn_raw_buffer_load_b128(rwo, wooff, ((i) / 5) * (4 * DM * 2) + ((i) % 5) * 256, 0); SB(); } while (0)
#define LDS_WAIT(PTR, VAL) do { int spin = 0; while (__hip_atomic_load(PTR, __ATOMIC_RELAXED, __HIP_MEMORY_SCOPE_WORKGROUP) < (VAL) && ++spin < SPIN_CAP) \
                                  __builtin_amdgcn_s_sleep(0); asm volatile("" ::: "memory"); SB(); } while (0)
#define LDS_DRAIN() do { SB(); asm volatile("s_waitcnt lgkmcnt(0)" ::: "memory"); SB(); } while (0)
#define POLL(COND, RELOAD) do { if (!__all(COND)) { int spin = 0; do { __builtin_amdgcn_s_sleep(1); asm volatile("" ::: "memory"); RELOAD; } \
                                  while (!__all(COND) && ++spin < SPIN_CAP); } } while (0)
#define OK2(G) ((((G)[0] & 0xffu) == tb_lo) & ((G)[1] == tb_hi) & (((G)[2] & 0xffu) == tb_lo) & ((G)[3] == tb_hi))

template <int CTRL> __device__ __forceinline__ unsigned dppu(unsigned x) { return (unsigned)__builtin_amdgcn_mov_dpp((int)x, CTRL, 0xf, 0xf, true); }
template <int CTRL> __device__ __forceinline__ float dppf(float x) { return __uint_as_float(dppu<CTRL>(__float_as_uint(x))); }
constexpr int XOR1 = 0xB1, XOR2 = 0x4E, XOR7 = 0x141, ROR8 = 0x128, ROR4 = 0x124, BCAST = 0x150;
__device__ __forceinline__ float dot2(unsigned a, unsigned b, float c) {
  return __builtin_amdgcn_fdot2_f32_bf16(__builtin_bit_cast(bf16x2, a), __builtin_bit_cast(bf16x2, b), c, false);
}
__device__ __forceinline__ unsigned pk_bf16(float lo, float hi) {
  bf16x2 v = {(__bf16)lo, (__bf16)hi};
  return __builtin_bit_cast(unsigned, v);
}
__device__ __forceinline__ unsigned split_hi_lo(float v) {
  const unsigned h = pk_bf16(v, 0.f) & 0xffffu;
  return pk_bf16(v, v - __uint_as_float(h << 16));
}
__device__ __forceinline__ float xrow_sum(float x) {
  auto s = __builtin_amdgcn_permlane16_swap(__float_as_uint(x), __float_as_uint(x), false, false);
  x = __uint_as_float(s[0]) + __uint_as_float(s[1]);
  auto t = __builtin_amdgcn_permlane32_swap(__float_as_uint(x), __float_as_uint(x), false, false);
  return __uint_as_float(t[0]) + __uint_as_float(t[1]);
}
__device__ __forceinline__ float xrow_max(float x) {
  auto s = __builtin_amdgcn_permlane16_swap(__float_as_uint(x), __float_as_uint(x), false, false);
  x = fmaxf(__uint_as_float(s[0]), __uint_as_float(s[1]));
  auto t = __builtin_amdgcn_permlane32_swap(__float_as_uint(x), __float_as_uint(x), false, false);
  return fmaxf(__uint_as_float(t[0]), __uint_as_float(t[1]));
}
__device__ __forceinline__ float row_sum(float x) { x += dppf<XOR1>(x); x += dppf<XOR2>(x); x += dppf<XOR7>(x); x += dppf<ROR8>(x); return x; }
__device__ __forceinline__ float row_max(float x) { x = fmaxf(x, dppf<XOR1>(x)); x = fmaxf(x, dppf<XOR2>(x)); x = fmaxf(x, dppf<XOR7>(x)); x = fmaxf(x, dppf<ROR8>(x)); return x; }

struct Sel { unsigned s[4]; };
__device__ __forceinline__ bf16x8 diagA(unsigned v, const Sel& sel) {
  u32x4 a = {__builtin_amdgcn_perm(v, v, sel.s[0]), __builtin_amdgcn_perm(v, v, sel.s[1]),
             __builtin_amdgcn_perm(v, v, sel.s[2]), __builtin_amdgcn_perm(v, v, sel.s[3])};
  return __builtin_bit_cast(bf16x8, a);
}
__device__ __forceinline__ f32x4 mfma(bf16x8 a, u32x4 b, f32x4 c) {
  return __builtin_amdgcn_mfma_f32_16x16x32_bf16(a, __builtin_bit_cast(bf16x8, b), c, 0, 0, 0);
}

__global__ __launch_bounds__(512, 2) void fused_kernel(
    const u16* __restrict__ x, const u16* __restrict__ kc, const u16* __restrict__ vc, const u16* __restrict__ wq,
    const u16* __restrict__ wo, const u16* __restrict__ wg, const u16* __restrict__ wu, const u16* __restrict__ wd,
    const u16* __restrict__ gw, u16* __restrict__ out, unsigned char* ws) {
  __shared__ __attribute__((aligned(16))) float s1buf[4 * 512];
  __shared__ __attribute__((aligned(16))) float pbuf[8 * 640];
  __shared__ __attribute__((aligned(16))) float obuf[8 * 132];
  __shared__ __attribute__((aligned(16))) u16 xs[4 * 160];
  __shared__ __attribute__((aligned(16))) unsigned ps[4 * 128];
  __shared__ __attribute__((aligned(16))) unsigned qs[64];
  __shared__ __attribute__((aligned(16))) float red[8 * 16];
  __shared__ __attribute__((aligned(16))) unsigned flg[16];
  unsigned cpf = 0;
  {
    unsigned long long pc;
    asm volatile("s_getpc_b64 %0" : "=s"(pc));
    const __amdgpu_buffer_rsrc_t rc = __builtin_amdgcn_make_buffer_rsrc((void*)(pc & ~255ull), 0, CODE_BYTES, 0x00020000);
    if (__builtin_amdgcn_readfirstlane(threadIdx.x) >= 384) cpf = __builtin_amdgcn_raw_buffer_load_b32(rc, (threadIdx.x - 384) * 128, 0, 0);
  }
  SB();
  if (threadIdx.x < 16) flg[threadIdx.x] = 0;
  WG_BARRIER();
  const int blk = blockIdx.x, tq = blk >> 3;
  const int h = 4 * (blk & 7) + (tq >> 3), b = (tq >> 1) & 3, half = tq & 1, j = 2 * h + half;
  const int tid = threadIdx.x, lane = tid & 63, R = lane >> 4, c = lane & 15;
  const int w = __builtin_amdgcn_readfirstlane(tid >> 6), w4 = w & 3;
  const __amdgpu_buffer_rsrc_t rs = __builtin_amdgcn_make_buffer_rsrc(ws, 0, WS_END, 0x00020000);
  const size_t kvoff = ((size_t)(b * NH + h) * SK + (SK - WIN) + half * 512) * DH + w4 * (KV_WOFF / 2);
  const __amdgpu_buffer_rsrc_t rkv = __builtin_amdgcn_make_buffer_rsrc((void*)((w < 4 ? kc : vc) + kvoff), 0, 512 * DH * 2 - w4 * KV_WOFF, 0x00020000);
  const __amdgpu_buffer_rsrc_t rwo = __builtin_amdgcn_make_buffer_rsrc((void*)(wo + (size_t)(h * DH + 16 * w) * DM), 0, 16 * DM * 2, 0x00020000);
  const unsigned wooff = (R * DM + 8 * c) * 2;
  u32x2 ev;
#define LDE() do { ev = __builtin_amdgcn_raw_buffer_load_b64(rs, WS_E + 8 * ((lane & 1) ? w4 : b), 0, 16); SB(); } while (0)
  unsigned tch = 0, tco = 0;
  u32x4 wr[20];
  f32x4 acc[5];
#pragma unroll
  for (int G = 0; G < 5; ++G) acc[G] = f32x4{0.f, 0.f, 0.f, 0.f};
  Sel sel;
  unsigned long long tb;
  unsigned tb_lo, tb_hi;
#define MAKE_SEL() do { const unsigned b0 = 2 * (c >> 3), pr = b0 | ((b0 + 1) << 8), e = c & 7; \
    const unsigned on = (e & 1) ? ((pr << 16) | 0x0C0Cu) : (0x0C0C0000u | pr); \
    _Pragma("unroll") for (int q = 0; q < 4; ++q) sel.s[q] = ((e >> 1) == (unsigned)q) ? on : 0x0C0C0C0Cu; } while (0)
#define MAKE_TAG() do { tb = (((unsigned long long)__builtin_amdgcn_readlane(ev[1], 0) << 32) | (unsigned)__builtin_amdgcn_readlane(ev[0], 0)) + 1; \
    tb_lo = (unsigned)tb & 0xffu; tb_hi = (unsigned)(tb >> 8); } while (0)

  if (w < 4) {
    __builtin_amdgcn_s_setprio(2);
    const bool has_s1 = (half == 0) || (h < 27);
    const int row0 = 160 * b + 40 * w;
    const int l40 = lane < 40 ? lane : 39;
    u32x4 s1[10], kr[32];
    LDK(0);
    const u32x2 xv = *reinterpret_cast<const u32x2*>(x + (l40 / 10) * DM + row0 + 4 * (l40 % 10));
    SB();
    {
      const int ld = half ? DF : NH * DH, hc = h < 27 ? h : 26;
      const u16* p = half ? ((c < 8 ? wg : wu) + 64 * hc + 8 * (c & 7)) : (wq + DH * h + 8 * c);
      p += (row0 + R) * ld;
#pragma unroll
      for (int i = 0; i < 10; ++i) s1[i] = *(const u32x4*)(p + 4 * i * ld);
    }
    SB();
    LDE();
    LDK(1); LDK(2); LDK(3);
    MAKE_SEL();
    if (lane < 40) {
      u16* d = &xs[w * 160 + (lane % 10) * 16 + lane / 10];
      d[0] = (u16)xv[0]; d[4] = (u16)(xv[0] >> 16); d[8] = (u16)xv[1]; d[12] = (u16)(xv[1] >> 16);
    }
    {
      f32x4 d0 = {0.f, 0.f, 0.f, 0.f}, d1 = d0;
#pragma unroll
      for (int i = 0; i < 10; ++i) {
        const u32x2 xx = *reinterpret_cast<const u32x2*>(&xs[w * 160 + (4 * i + R) * 4]);
        d0 = mfma(diagA(xx[0], sel), s1[i], d0); d1 = mfma(diagA(xx[1], sel), s1[i], d1);
      }
      float* dst = &s1buf[w * 512 + (R >> 1) * 128 + 8 * c + 4 * (R & 1)];
      *reinterpret_cast<f32x4*>(dst) = d0; *reinterpret_cast<f32x4*>(dst + 256) = d1;
    }
    LDS_DRAIN();
    if (lane == 0) __hip_atomic_fetch_add(&flg[0], 1u, __ATOMIC_RELAXED, __HIP_MEMORY_SCOPE_WORKGROUP);
    MAKE_TAG();
    const unsigned long long tw = (((unsigned long long)__builtin_amdgcn_readlane(ev[1], 1) << 32) | (unsigned)__builtin_amdgcn_readlane(ev[0], 1)) + 1;
    LDS_WAIT(&flg[0], 4u);
    if (has_s1) {
      f32x2 a = {0.f, 0.f};
#pragma unroll
      for (int q = 0; q < 4; ++q) a += *reinterpret_cast<const f32x2*>(&s1buf[q * 512 + w * 128 + 2 * lane]);
      const unsigned lo = (unsigned)tw & 0xffu, hi = (unsigned)(tw >> 8);
      const u32x4 g = {(__float_as_uint(a[0]) & ~0xffu) | lo, hi, (__float_as_uint(a[1]) & ~0xffu) | lo, hi};
      const int tg = half ? 32 + h : h;
      __builtin_amdgcn_raw_buffer_store_b128(g, rs, WS_S1 + (tg * 4 + w) * 4096 + ((lane & 3) * 64 + b * 16 + (lane >> 2)) * 16, 0, 16);
    }
    SB();
#pragma unroll
    for (int i = 0; i < 6; ++i) { LDK(4 + 3 * i); LDK(5 + 3 * i); LDK(6 + 3 * i); LDW(i); }
    const u32x4 gx0 = *reinterpret_cast<const u32x4*>(x + b * DM + 16 * l40), gx1 = *reinterpret_cast<const u32x4*>(x + b * DM + 16 * l40 + 8);
    const u32x4 gg0 = *reinterpret_cast<const u32x4*>(gw + 16 * l40), gg1 = *reinterpret_cast<const u32x4*>(gw + 16 * l40 + 8);
    SB();
    const unsigned qoff = WS_S1 + (h * 4 + b) * 4096 + w * 1024 + lane * 16;
    u32x4 qg;
#define LDQ() do { qg = __builtin_amdgcn_raw_buffer_load_b128(rs, qoff, 0, 16); } while (0)
    LDQ();
    SB();
    tch = __builtin_amdgcn_raw_buffer_load_b32(rs, j < 40 ? ((lane & 3) == 0 ? WS_FG + ((b * 64 + 16 * w + (lane >> 2)) * 640 + 16 * j) * 8 : OOB)
                                                          : (lane == 0 ? WS_FG + ((b * 64 + j) * 640 + 208 * w) * 8 : OOB), 0, 16);
    tco = __builtin_amdgcn_raw_buffer_load_b32(__builtin_amdgcn_make_buffer_rsrc(out, 0, 4 * DM * 2, 0x00020000),
                                               (j < 40 && tid == 0) ? (b * DM + 16 * j) * 2 : OOB, 0, 16);
    SB();
    LDK(22); LDK(23); LDK(24); LDK(25); LDK(26); LDW(6); LDK(27); LDK(28); LDK(29); LDK(30); LDK(31); LDW(7);
    float gate;
    {
      float a = 0.f;
#pragma unroll
      for (int q = 0; q < 4; ++q) { a = dot2(gx0[q], gg0[q], a); a = dot2(gx1[q], gg1[q], a); }
      a = lane < 40 ? a : 0.f;
      a = xrow_sum(row_sum(a));
      gate = 1.f / (1.f + __expf(-a));
    }
    POLL(OK2(qg), LDQ());
    {
      const unsigned qq = pk_bf16(xrow_sum(__uint_as_float(qg[0] & ~0xffu)), xrow_sum(__uint_as_float(qg[2] & ~0xffu)));
      if (lane < 16) qs[4 * lane + w] = qq;
    }
    LDS_DRAIN();
    if (lane == 0) __hip_atomic_fetch_add(&flg[1], 1u, __ATOMIC_RELAXED, __HIP_MEMORY_SCOPE_WORKGROUP);
    LDS_WAIT(&flg[1], 4u);
    const u32x4 q4 = *reinterpret_cast<const u32x4*>(&qs[4 * c]);
    float sc[2];
    const bool b8 = c & 8, b4 = c & 4, b2 = c & 2, b1 = c & 1;
#pragma unroll
    for (int t = 0; t < 2; ++t) {
      float s[16];
#pragma unroll
      for (int i = 0; i < 16; ++i) {
        float a = dot2(kr[16 * t + i][0], q4[0], 0.f);
        a = dot2(kr[16 * t + i][1], q4[1], a); a = dot2(kr[16 * t + i][2], q4[2], a); s[i] = dot2(kr[16 * t + i][3], q4[3], a);
      }
#pragma unroll
      for (int i = 0; i < 8; ++i) s[i] = (b8 ? s[i + 8] : s[i]) + dppf<ROR8>(b8 ? s[i] : s[i + 8]);
#pragma unroll
      for (int i = 0; i < 4; ++i) s[i] = (b4 ? s[i + 4] : s[i]) + dppf<XOR7>(b4 ? s[i] : s[i + 4]);
#pragma unroll
      for (int i = 0; i < 2; ++i) s[i] = (b2 ? s[i + 2] : s[i]) + dppf<XOR2>(b2 ? s[i] : s[i + 2]);
      sc[t] = ((b1 ? s[1] : s[0]) + dppf<XOR1>(b1 ? s[0] : s[1])) * (0.08838834764831845f * 1.4426950408889634f);
      SB();
    }
    const float mw = xrow_max(row_max(fmaxf(sc[0], sc[1])));
    const float p0 = __builtin_amdgcn_exp2f(sc[0] - mw), p1 = __builtin_amdgcn_exp2f(sc[1] - mw);
    *reinterpret_cast<u32x2*>(&ps[w * 128 + 2 * lane]) = u32x2{split_hi_lo(p0), split_hi_lo(p1)};
    LDS_DRAIN();
    if (lane == 0) __hip_atomic_store(&flg[4 + w], 1u, __ATOMIC_RELAXED, __HIP_MEMORY_SCOPE_WORKGROUP);
    const float lw = xrow_sum(row_sum(p0 + p1));
    const unsigned mlbase = WS_ML + (b * NH + h) * 256, tb32 = (unsigned)tb;
    if (lane == 0) {
      const u32x4 g = {__float_as_uint(mw), tb32, __float_as_uint(lw), tb32};
      __builtin_amdgcn_raw_buffer_store_b128(g, rs, mlbase + (half * 4 + w) * 16, 0, 16);
    }
    SB();
    const int fl = 8 * w + 4 * (c >> 3) + R, f = 27 * j + (fl < 27 ? fl : 26);
    const int hcol = ((c >> 2) & 1) * 64 + (f & 63);
    const unsigned hoff = WS_S1 + ((32 + (f >> 6)) * 4 + b) * 4096 + (((hcol >> 1) & 3) * 64 + (c & 3) * 16 + (hcol >> 3)) * 16 + (hcol & 1) * 8;
    u32x2 hg = __builtin_amdgcn_raw_buffer_load_b64(rs, hoff, 0, 16);
    u32x4 dr[10];
    {
      const int f0 = 8 * w + R, f1 = f0 + 4;
      const __amdgpu_buffer_rsrc_t rwd = __builtin_amdgcn_make_buffer_rsrc((void*)(wd + (size_t)27 * j * DM), 0, 27 * DM * 2, 0x00020000);
      const unsigned d0 = ((f0 < 27 ? f0 : 26) * DM + 8 * c) * 2, d1 = ((f1 < 27 ? f1 : 26) * DM + 8 * c) * 2;
#pragma unroll
      for (int G = 0; G < 5; ++G) dr[G] = __builtin_amdgcn_raw_buffer_load_b128(rwd, d0, G * 256, 0);
#pragma unroll
      for (int G = 0; G < 5; ++G) dr[5 + G] = __builtin_amdgcn_raw_buffer_load_b128(rwd, d1, G * 256, 0);
    }
    SB();
    LDW(8); LDW(9); LDW(10); LDW(11); LDW(12); LDW(13);
    u32x4 mg = __builtin_amdgcn_raw_buffer_load_b128(rs, mlbase + (c & 7) * 16, 0, 16);
    SB();
    LDW(14); LDW(15); LDW(16); LDW(17); LDW(18); LDW(19);
    {
      POLL(((hg[0] & 0xffu) == tb_lo) & (hg[1] == tb_hi), hg = __builtin_amdgcn_raw_buffer_load_b64(rs, hoff, 0, 16));
      float a = __uint_as_float(hg[0] & ~0xffu);
      a += dppf<XOR1>(a); a += dppf<XOR2>(a);
      const float other = dppf<XOR7>(a);
      const float gv = (c & 4) ? other : a, uv = (c & 4) ? a : other;
      const float z2 = 1.5957691216057308f * (gv + 0.044715f * gv * gv * gv);
      float hv = gv / (1.f + __expf(-z2)) * uv * (1.f - gate);
      hv = fl < 27 ? hv : 0.f;
      const unsigned hh = split_hi_lo(hv);
      const bf16x8 A0 = diagA(dppu<BCAST + 0>(hh), sel), A1 = diagA(dppu<BCAST + 8>(hh), sel);
#pragma unroll
      for (int G = 0; G < 5; ++G) acc[G] = mfma(A0, dr[G], acc[G]);
#pragma unroll
      for (int G = 0; G < 5; ++G) acc[G] = mfma(A1, dr[5 + G], acc[G]);
    }
    SB();
    POLL((mg[1] == tb32) & (mg[3] == tb32), mg = __builtin_amdgcn_raw_buffer_load_b128(rs, mlbase + (c & 7) * 16, 0, 16));
    {
      const float mi = __uint_as_float(mg[0]), li = __uint_as_float(mg[2]);
      float M = fmaxf(mi, dppf<XOR1>(mi)); M = fmaxf(M, dppf<XOR2>(M)); M = fmaxf(M, dppf<XOR7>(M));
      float L = li * __builtin_amdgcn_exp2f(mi - M);
      L += dppf<XOR1>(L); L += dppf<XOR2>(L); L += dppf<XOR7>(L);
      if (lane == 0) flg[8 + w] = __float_as_uint(__builtin_amdgcn_exp2f(mw - M) / L * gate);
    }
  } else {
    u32x4 vr[32];
#define VG(i) do { LDV(2 * (i)); LDV(2 * (i) + 1); LDW(i); } while (0)
#define PV2(I) do { o0 = mfma(diagA(dppu<BCAST + ((I) & 15)>(pp[(I) >> 4]), sel), vr[I], o0); \
                    o1 = mfma(diagA(dppu<BCAST + (((I) + 1) & 15)>(pp[(I) >> 4]), sel), vr[(I) + 1], o1); SB(); } while (0)
    VG(0); LDE(); VG(1); VG(2); VG(3); VG(4); VG(5); VG(6); VG(7); VG(8); VG(9); VG(10); VG(11); LDV(24); LDV(25); LDV(26);
    MAKE_SEL();
    MAKE_TAG();
    asm volatile("" :: "v"(cpf));
    LDS_WAIT(&flg[w], 1u);
    const u32x2 pp = *reinterpret_cast<const u32x2*>(&ps[w4 * 128 + 2 * lane]);
    f32x4 o0 = {0.f, 0.f, 0.f, 0.f}, o1 = o0;
    PV2(0); PV2(2); PV2(4); PV2(6);
    LDV(27); LDV(28); LDV(29);
    PV2(8); PV2(10); PV2(12);
    LDV(30); LDV(31); LDW(12);
    PV2(14); PV2(16); PV2(18);
    LDW(13); LDW(14); LDW(15);
    PV2(20); PV2(22);
    LDW(16); LDW(17); LDW(18); LDW(19);
    PV2(24); PV2(26); PV2(28); PV2(30);
    *reinterpret_cast<f32x4*>(&obuf[(2 * w4 + (R >> 1)) * 132 + 8 * c + 4 * (R & 1)]) = o0 + o1;
  }
  const float* op = &obuf[(c >> 2) * 2 * 132 + 16 * w + 4 * (c & 3) + R];
  float* pw = &pbuf[w * 640 + 8 * c + 4 * (R & 1) + 2 * (R >> 1)];
  WG_BARRIER();
  {
    float o = (op[0] + op[132]) * __uint_as_float(flg[8 + (c >> 2)]);
    o += dppf<ROR4>(o); o += dppf<ROR8>(o);
    const unsigned ov = split_hi_lo(o);
#define WO_STEP(RG) { const bf16x8 A = diagA(dppu<(RG) * 0x55>(ov), sel); \
    _Pragma("unroll") for (int G = 0; G < 5; ++G) acc[G] = mfma(A, wr[5 * (RG) + G], acc[G]); }
    WO_STEP(0) WO_STEP(1) WO_STEP(2) WO_STEP(3)
    SB();
  }
#define PART_OUT() do { _Pragma("unroll") for (int G = 0; G < 5; ++G) { \
      auto u = __builtin_amdgcn_permlane32_swap(__float_as_uint(acc[G][0]), __float_as_uint(acc[G][2]), false, false); \
      auto v = __builtin_amdgcn_permlane32_swap(__float_as_uint(acc[G][1]), __float_as_uint(acc[G][3]), false, false); \
      const f32x2 r = {__uint_as_float(u[0]) + __uint_as_float(u[1]), __uint_as_float(v[0]) + __uint_as_float(v[1])}; \
      *reinterpret_cast<f32x2*>(pw + 128 * G) = r; } \
    WG_BARRIER(); } while (0)
  PART_OUT();
  if (w < 5) {
    f32x2 a = {0.f, 0.f};
#pragma unroll
    for (int q = 0; q < 8; ++q) a += *reinterpret_cast<const f32x2*>(&pbuf[q * 640 + 2 * tid]);
    const u32x4 g = {(__float_as_uint(a[0]) & ~0xffu) | tb_lo, tb_hi, (__float_as_uint(a[1]) & ~0xffu) | tb_lo, tb_hi};
    __builtin_amdgcn_raw_buffer_store_b128(g, rs, WS_FG + ((b * 64 + j) * 640 + 2 * tid) * 8, 0, 16);
  }
  SB();
  asm volatile("" :: "v"(tch), "v"(tco));
  if (j < 40) {
    unsigned off = WS_FG + ((b * 64 + 8 * w + (lane >> 3)) * 640 + 16 * j + 2 * (lane & 7)) * 8, x0 = 0, x1 = 0;
    int spin = 0;
#pragma clang loop unroll(disable)
    do {
      asm volatile("" ::: "memory");
      const u32x4 t = __builtin_amdgcn_raw_buffer_load_b128(rs, off, 0, 16);
      SB();
      const bool good = OK2(t) & (off != OOB);
      x0 = good ? t[0] : x0; x1 = good ? t[2] : x1; off = good ? OOB : off;
    } while (__ballot(off != OOB) && ++spin < SPIN_CAP);
    float a0 = __uint_as_float(x0 & ~0xffu), a1 = __uint_as_float(x1 & ~0xffu);
    a0 += dppf<ROR8>(a0); a1 += dppf<ROR8>(a1);
    a0 = xrow_sum(a0); a1 = xrow_sum(a1);
    if (lane < 8) *reinterpret_cast<f32x2*>(&red[w * 16 + 2 * lane]) = f32x2{a0, a1};
    LDS_DRAIN();
    unsigned ticket = 0;
    if (lane == 0) ticket = __hip_atomic_fetch_add(&flg[2], 1u, __ATOMIC_RELAXED, __HIP_MEMORY_SCOPE_WORKGROUP);
    if (__builtin_amdgcn_readfirstlane(ticket) == 7) {
      asm volatile("" ::: "memory");
      const f32x2 r = *reinterpret_cast<const f32x2*>(&red[(lane >> 3) * 16 + 2 * (lane & 7)]);
      a0 = r[0] + dppf<ROR8>(r[0]); a1 = r[1] + dppf<ROR8>(r[1]);
      a0 = xrow_sum(a0); a1 = xrow_sum(a1);
      if (lane < 8) *reinterpret_cast<unsigned*>(out + b * DM + 16 * j + 2 * lane) = pk_bf16(a0, a1);
      if (j == 0 && lane == 0) {
        const u32x2 e = {(unsigned)tb, (unsigned)(tb >> 32)};
        __builtin_amdgcn_raw_buffer_store_b64(e, rs, WS_E + 8 * b, 0, 16);
      }
    }
  }
}

extern "C" void kernel_launch(void* const* d_in, const int* in_sizes, int n_in, void* d_out, int out_size, void* d_ws,
                              size_t ws_size, hipStream_t stream) {
  fused_kernel<<<256, 512, 0, stream>>>((const u16*)d_in[0], (const u16*)d_in[1], (const u16*)d_in[2], (const u16*)d_in[3],
                                        (const u16*)d_in[4], (const u16*)d_in[6], (const u16*)d_in[7], (const u16*)d_in[8],
                                        (const u16*)d_in[9], (u16*)d_out, (unsigned char*)d_ws);
}
```
